```python
import torch

def _softmax(x, axis=-1):
    return torch.softmax(x, dim=axis)
def _gelu(x):
    return torch.nn.functional.gelu(x, approximate="tanh")
def _silu(x):
    return torch.nn.functional.silu(x)
def _sigmoid(x):
    return torch.sigmoid(x)
def _layernorm(x, axis=-1, eps=1e-5):
    m = x.mean(dim=axis, keepdim=True)
    v = x.var(dim=axis, keepdim=True, unbiased=False)
    return (x - m) / torch.sqrt(v + eps)
def _scatter_add(tgt, idx, src, axis=0):
    return tgt.clone().index_add_(axis, idx, src)
def _segment_sum(src, seg, n):
    out = torch.zeros((n,) + src.shape[1:], dtype=src.dtype)
    return out.index_add_(0, seg, src)
def _pad(x, pw):
    flat = tuple(v for pair in reversed(pw) for v in pair)
    return torch.nn.functional.pad(x, flat)

kernel_name = 'sequential_preLN__linear_attn_block__mixer_channel_mlp__prompt'

def setup_inputs():

    _w = (lambda a: (lambda t: t.to(torch.bfloat16) if t.is_floating_point() else t)(torch.as_tensor(a)))
    def _A_setup():
        B, S, D = 1, 2048, 512
        return {
            "x": _w((lambda *s: torch.randn(*s, dtype=torch.bfloat16) * 0.05)(B, S, D)),
            "wqkv": _w((lambda *s: torch.randn(*s, dtype=torch.bfloat16) * 0.1)(D, 3 * D)),
            "wo": _w((lambda *s: torch.randn(*s, dtype=torch.bfloat16) * 0.05)(D, D)),
        }
    _A_in = _A_setup()

    def _B_setup():
        B, S, D, F = 1, 2048, 512, 1792
        return {
            "x": _w((lambda *s: torch.randn(*s, dtype=torch.bfloat16) * 0.05)(B, S, D)),
            "w1": _w((lambda *s: torch.randn(*s, dtype=torch.bfloat16) * 3.2)(D, F) * (D ** -0.5)),
            "b1": _w((lambda *s: torch.randn(*s, dtype=torch.bfloat16) * 0.8)(F)),
            "w2": _w((lambda *s: torch.randn(*s, dtype=torch.bfloat16) * 0.05)(F, D) * (F ** -0.5)),
            "b2": _w((lambda *s: torch.randn(*s, dtype=torch.bfloat16) * 0.05)(D)),
        }
    _B_in = _B_setup()

    return {
        "A_x": _A_in["x"],
        "A_wqkv": _A_in["wqkv"],
        "A_wo": _A_in["wo"],
        "B_x": _B_in["x"],
        "B_w1": _B_in["w1"],
        "B_b1": _B_in["b1"],
        "B_w2": _B_in["w2"],
        "B_b2": _B_in["b2"],
    }

def reference(A_x, A_wqkv, A_wo, B_x, B_w1, B_b1, B_w2, B_b2):
    def _A_ref(x, wqkv, wo):
        B, S, D = x.shape
        H = 32; Dh = D // H
        qkv = (lambda x, *d: x.permute(*d))((x @ wqkv).reshape(B, S, 3, H, Dh), 2, 0, 3, 1, 4)
        q, k, v = qkv[0], qkv[1], qkv[2]
        phi = lambda t: torch.where(t > 0, t + 1, torch.exp(t)) + 1e-6
        pq, pk = phi(q), phi(k)
        kv = torch.einsum("bhsd,bhse->bhde", pk, v)
        z = torch.einsum("bhsd->bhd", pk)
        num = torch.einsum("bhqd,bhde->bhqe", pq, kv)
        den = torch.einsum("bhqd,bhd->bhq", pq, z)[..., None] + 1e-6
        o = num / den
        return (lambda x, *d: x.permute(*d))(o, 0, 2, 1, 3).reshape(B, S, D) @ wo

    def _B_ref(x, w1, b1, w2, b2):
        y = _layernorm(x, axis=-1)
        h = _gelu(torch.einsum("bsd,df->bsf", y, w1) + b1)
        return torch.einsum("bsf,fd->bsd", h, w2) + b2

    x = A_x
    h = x + _A_ref(_layernorm(x, axis=-1), A_wqkv, A_wo)
    h2 = h + _B_ref(_layernorm(h, axis=-1), B_w1, B_b1, B_w2, B_b2)
    return h2
```

```cpp
#include <hip/hip_runtime.h>
#include <stdint.h>
namespace nv {
typedef unsigned short bf16_t;
__device__ __forceinline__ float b2f(bf16_t v) { return __uint_as_float(((unsigned)v) << 16); }
__device__ __forceinline__ bf16_t f2b(float f) { unsigned u = __float_as_uint(f); u += 0x7fff + ((u >> 16) & 1); return (bf16_t)(u >> 16); }
__device__ __forceinline__ float rb(float f) { return b2f(f2b(f)); }

__global__ void ln_k(const bf16_t* x, bf16_t* y) {
  int row = blockIdx.x; int t = threadIdx.x;
  __shared__ float red[256];
  float a = b2f(x[row*512 + t]), b = b2f(x[row*512 + 256 + t]);
  red[t] = a + b; __syncthreads();
  for (int s = 128; s > 0; s >>= 1) { if (t < s) red[t] += red[t+s]; __syncthreads(); }
  float mean = red[0] / 512.f; __syncthreads();
  red[t] = (a-mean)*(a-mean) + (b-mean)*(b-mean); __syncthreads();
  for (int s = 128; s > 0; s >>= 1) { if (t < s) red[t] += red[t+s]; __syncthreads(); }
  float var = red[0] / 512.f;
  float m = rb(mean), v = rb(var);
  float sd = rb(sqrtf(rb(v + 1e-5f)));
  y[row*512 + t] = f2b(rb(a - m) / sd);
  y[row*512 + 256 + t] = f2b(rb(b - m) / sd);
}
__global__ void gemm_k(const bf16_t* A, const bf16_t* B, bf16_t* C, const bf16_t* bias, const bf16_t* res, int M, int N, int K, int mode) {
  int n = blockIdx.x * 64 + (threadIdx.x & 63); int m = blockIdx.y * 4 + (threadIdx.x >> 6);
  if (n >= N || m >= M) return;
  float acc = 0.f;
  for (int k = 0; k < K; ++k) acc += b2f(A[(size_t)m*K + k]) * b2f(B[(size_t)k*N + n]);
  float v = rb(acc);
  if (mode == 1) { v = rb(v + b2f(bias[n])); float u = 0.7978845608028654f * (v + 0.044715f * v*v*v); v = rb(0.5f * v * (1.f + tanhf(u))); }
  else if (mode == 2) { v = rb(b2f(res[(size_t)m*N + n]) + v); }
  else if (mode == 3) { v = rb(v + b2f(bias[n])); v = rb(b2f(res[(size_t)m*N + n]) + v); }
  C[(size_t)m*N + n] = f2b(v);
}
__device__ __forceinline__ float phi(float t) { float p = t > 0.f ? rb(t + 1.f) : rb(expf(t)); return rb(p + 1e-6f); }
__global__ void kv_k(const bf16_t* qkv, float* kv, float* z) {
  int h = blockIdx.x; int t = threadIdx.x;
  if (t < 256) { int d = t >> 4, e = t & 15; float acc = 0.f;
    for (int s = 0; s < 2048; ++s) acc += phi(b2f(qkv[s*1536 + 512 + h*16 + d])) * b2f(qkv[s*1536 + 1024 + h*16 + e]);
    kv[h*256 + t] = rb(acc);
  } else if (t < 272) { int d = t - 256; float acc = 0.f;
    for (int s = 0; s < 2048; ++s) acc += phi(b2f(qkv[s*1536 + 512 + h*16 + d]));
    z[h*16 + d] = rb(acc);
  }
}
__global__ void o_k(const bf16_t* qkv, const float* kv, const float* z, bf16_t* o) {
  int s = blockIdx.x; int t = threadIdx.x;
  int h = t >> 4, e = t & 15; float num = 0.f, den = 0.f;
  for (int d = 0; d < 16; ++d) { float p = phi(b2f(qkv[s*1536 + h*16 + d])); num += p * kv[h*256 + d*16 + e]; den += p * z[h*16 + d]; }
  num = rb(num); den = rb(rb(den) + 1e-6f);
  o[s*512 + t] = f2b(num / den);
}
static void run(void* const* d_in, void* d_out, void* d_ws, hipStream_t stream) {
  const bf16_t* x = (const bf16_t*)d_in[0]; const bf16_t* wqkv = (const bf16_t*)d_in[1]; const bf16_t* wo = (const bf16_t*)d_in[2];
  const bf16_t* w1 = (const bf16_t*)d_in[4]; const bf16_t* b1 = (const bf16_t*)d_in[5]; const bf16_t* w2 = (const bf16_t*)d_in[6]; const bf16_t* b2 = (const bf16_t*)d_in[7];
  char* ws = (char*)d_ws;
  bf16_t* y = (bf16_t*)(ws); bf16_t* qkv = (bf16_t*)(ws + (4<<20)); float* kv = (float*)(ws + (12<<20)); float* z = (float*)(ws + (13<<20));
  bf16_t* o = (bf16_t*)(ws + (16<<20)); bf16_t* h = (bf16_t*)(ws + (20<<20)); bf16_t* y1 = (bf16_t*)(ws + (24<<20)); bf16_t* y2 = (bf16_t*)(ws + (28<<20)); bf16_t* hh = (bf16_t*)(ws + (32<<20));
  ln_k<<<2048, 256, 0, stream>>>(x, y);
  gemm_k<<<dim3(1536/64, 512), 256, 0, stream>>>(y, wqkv, qkv, nullptr, nullptr, 2048, 1536, 512, 0);
  kv_k<<<32, 320, 0, stream>>>(qkv, kv, z);
  o_k<<<2048, 512, 0, stream>>>(qkv, kv, z, o);
  gemm_k<<<dim3(512/64, 512), 256, 0, stream>>>(o, wo, h, nullptr, x, 2048, 512, 512, 2);
  ln_k<<<2048, 256, 0, stream>>>(h, y1);
  ln_k<<<2048, 256, 0, stream>>>(y1, y2);
  gemm_k<<<dim3(1792/64, 512), 256, 0, stream>>>(y2, w1, hh, b1, nullptr, 2048, 1792, 512, 1);
  gemm_k<<<dim3(512/64, 512), 256, 0, stream>>>(hh, w2, (bf16_t*)d_out, b2, h, 2048, 512, 1792, 3);
}
}
extern "C" void kernel_launch(void* const* d_in, const int* in_sizes, int n_in, void* d_out, int out_size, void* d_ws, size_t ws_size, hipStream_t stream) {
  nv::run(d_in, d_out, d_ws, stream);
}
```

```cpp
#include <hip/hip_runtime.h>
#include <stdint.h>

typedef unsigned short bf16_t;
typedef __attribute__((ext_vector_type(8))) short s16x8;
typedef __attribute__((ext_vector_type(4))) short s16x4;
typedef __attribute__((ext_vector_type(4))) float f32x4;
typedef __attribute__((ext_vector_type(2))) float f32x2;
typedef __attribute__((ext_vector_type(4))) unsigned u32x4;
typedef __attribute__((ext_vector_type(2))) unsigned u32x2;
#define LAS __attribute__((address_space(3)))
#define DI __device__ __forceinline__

constexpr size_t WS_KVP   = 1u << 20;
constexpr size_t WS_PQ    = 2u << 20;
constexpr size_t WS_O     = 4u << 20;
constexpr size_t WS_H     = 6u << 20;
constexpr size_t WS_HST   = 8u << 20;
constexpr size_t WS_HH    = 10u << 20;
constexpr int KVP_STRIDE = 2176;

DI float b2f(unsigned short v) { return __uint_as_float(((unsigned)v) << 16); }
DI unsigned pk_bf16(float lo, float hi) { unsigned r; asm("v_cvt_pk_bf16_f32 %0, %1, %2" : "=v"(r) : "v"(lo), "v"(hi)); return r; }
DI float lo_f(unsigned u) { return __uint_as_float(u << 16); }
DI float hi_f(unsigned u) { return __uint_as_float(u & 0xffff0000u); }

DI void glds16(const void* sbase, unsigned voff, unsigned lds_dst) {
  unsigned keep;
  asm volatile("s_mov_b32 %0, m0\n\ts_mov_b32 m0, %3\n\ts_nop 0\n\tglobal_load_lds_dwordx4 %1, %2\n\ts_mov_b32 m0, %0"
               : "=&s"(keep) : "v"(voff), "s"(sbase), "s"(lds_dst) : "memory");
}
#define WAIT_VM(n) asm volatile("s_waitcnt vmcnt(" #n ")" ::: "memory")
#define WAIT_LGKM0() asm volatile("s_waitcnt lgkmcnt(0)" ::: "memory")
DI void bar() { asm volatile("s_waitcnt lgkmcnt(0)" ::: "memory"); __builtin_amdgcn_s_barrier(); asm volatile("" ::: "memory"); }

DI s16x8 tr_frag(LAS char* lds, unsigned a0, unsigned a1) {
  s16x4 a = __builtin_amdgcn_ds_read_tr16_b64_v4i16((LAS s16x4*)(lds + a0));
  s16x4 b = __builtin_amdgcn_ds_read_tr16_b64_v4i16((LAS s16x4*)(lds + a1));
  return __builtin_shufflevector(a, b, 0, 1, 2, 3, 4, 5, 6, 7);
}
DI f32x4 mfma32(s16x8 a, s16x8 b, f32x4 c) { return __builtin_amdgcn_mfma_f32_16x16x32_bf16(a, b, c, 0, 0, 0); }
DI f32x4 mfma16(s16x4 a, s16x4 b, f32x4 c) { return __builtin_amdgcn_mfma_f32_16x16x16bf16_1k(a, b, c, 0, 0, 0); }
DI s16x4 pack4(float a, float b, float c, float d) { u32x2 u; u.x = pk_bf16(a, b); u.y = pk_bf16(c, d); return __builtin_bit_cast(s16x4, u); }
DI float phi(float t) { return (t > 0.f ? t + 1.f : __expf(t)) + 1e-6f; }

struct Ids { int p, c; };
DI Ids ids() { int b = blockIdx.x; int xcd = b & 7, idx = b >> 3; Ids r; r.p = xcd * 2 + (idx >> 4); r.c = idx & 15; return r; }

__global__ __launch_bounds__(512, 2) void k1_qkv(const bf16_t* __restrict__ x, const bf16_t* __restrict__ wqkv, char* __restrict__ ws) {
  extern __shared__ __attribute__((aligned(16))) char smem_[];
  LAS char* lds = (LAS char*)smem_;
  const int tid = threadIdx.x, lane = tid & 63;
  const int w = __builtin_amdgcn_readfirstlane(tid >> 6);
  const Ids id = ids();
  const int g = lane >> 4, r = lane & 15, q = (lane >> 2) & 3, pp = lane & 3;
  constexpr unsigned L_WKV = 0, L_WQ = 65536, L_X = 98304;
  const unsigned ldsb = (unsigned)(size_t)smem_;

  const int xrow = lane >> 3;
  const unsigned xoff = (unsigned)(((id.p * 128 + w * 16 + xrow) * 512) * 2 + (((lane & 7) ^ (xrow & 7)) * 16));
  const int wk = lane >> 2;
  const unsigned wcol = (unsigned)((((((lane >> 1) & 1) ^ ((wk >> 3) & 1)) * 16) + (lane & 1) * 8) * 2);
  const unsigned woff = (unsigned)(wk * 1536 * 2) + wcol + (unsigned)(id.c * 64);
  auto issue_x = [&](int ch) {
    unsigned dst = ldsb + L_X + w * 8192 + (ch & 3) * 2048;
    glds16(x, xoff + ch * 128, dst);
    glds16(x, xoff + ch * 128 + 8 * 1024, dst + 1024);
  };
  auto issue_wkv = [&](int ch) {
    int sub = w >> 1, seg = w & 1;
    glds16(wqkv, woff + (unsigned)((ch * 64 + sub * 16) * 3072 + (1 + seg) * 1024), ldsb + L_WKV + ch * 8192 + w * 1024);
  };
  auto issue_wq = [&](int i) {
    int blk = w * 4 + i;
    glds16(wqkv, woff + (unsigned)(blk * 16 * 3072), ldsb + L_WQ + blk * 1024);
  };
#pragma unroll
  for (int ch = 0; ch < 4; ++ch) { issue_x(ch); issue_wkv(ch); }
#pragma unroll
  for (int ch = 4; ch < 8; ++ch) issue_wkv(ch);
#pragma unroll
  for (int i = 0; i < 4; ++i) issue_wq(i);

  const unsigned xrd = L_X + w * 8192 + r * 128;
  const unsigned wrd = (unsigned)((g >> 1) * 1024 + (8 * (g & 1) + q) * 64 + 8 * pp);
  const unsigned ct0 = 32u * (0 ^ (g & 1)), ct1 = 32u * (1 ^ (g & 1));

  s16x8 ones; for (int j = 0; j < 8; ++j) ones[j] = (short)0x3f80;
  s16x8 xf[16];
  f32x4 acc[4];
  f32x4 cs[4], sx, gx;
#pragma unroll
  for (int i = 0; i < 4; ++i) { acc[i] = (f32x4){0, 0, 0, 0}; cs[i] = (f32x4){0, 0, 0, 0}; }
  sx = (f32x4){0, 0, 0, 0}; gx = (f32x4){0, 0, 0, 0};

#pragma unroll
  for (int ch = 0; ch < 8; ++ch) {
    if (ch == 0) WAIT_VM(17); else if (ch == 1) WAIT_VM(16); else if (ch == 2) WAIT_VM(15); else if (ch == 3) WAIT_VM(14);
    else if (ch == 4) WAIT_VM(6); else if (ch == 5) WAIT_VM(4); else if (ch == 6) WAIT_VM(2); else WAIT_VM(0);
    bar();
#pragma unroll
    for (int s = 0; s < 2; ++s)
      xf[ch * 2 + s] = *(LAS s16x8*)(lds + xrd + (ch & 3) * 2048 + (((4 * s + g) ^ (r & 7)) * 16));
    if (ch < 4) { WAIT_LGKM0(); issue_x(ch + 4); }
#pragma unroll
    for (int s = 0; s < 2; ++s) {
      const s16x8 xa = xf[ch * 2 + s];
#pragma unroll
      for (int seg = 0; seg < 2; ++seg) {
        const unsigned base = L_WKV + ch * 8192 + s * 4096 + seg * 1024 + (g >> 1) * 1024   + wrd;
        s16x8 f0 = tr_frag(lds, base + ct0, base + ct0 + 256);
        s16x8 f1 = tr_frag(lds, base + ct1, base + ct1 + 256);
        acc[seg * 2 + 0] = mfma32(xa, f0, acc[seg * 2 + 0]);
        acc[seg * 2 + 1] = mfma32(xa, f1, acc[seg * 2 + 1]);
        if (ch == w) { cs[seg * 2 + 0] = mfma32(ones, f0, cs[seg * 2 + 0]); cs[seg * 2 + 1] = mfma32(ones, f1, cs[seg * 2 + 1]); }
      }
      sx = mfma32(xa, ones, sx);
      gx = mfma32(xa, xa, gx);
    }
  }
  f32x4 aq[2], csq[2];
  aq[0] = aq[1] = csq[0] = csq[1] = (f32x4){0, 0, 0, 0};
#pragma unroll
  for (int kb = 0; kb < 16; ++kb) {
    const unsigned base = L_WQ + kb * 2048 + wrd;
    s16x8 f0 = tr_frag(lds, base + ct0, base + ct0 + 256);
    s16x8 f1 = tr_frag(lds, base + ct1, base + ct1 + 256);
    aq[0] = mfma32(f0, xf[kb], aq[0]);
    aq[1] = mfma32(f1, xf[kb], aq[1]);
    if ((kb >> 1) == w) { csq[0] = mfma32(f0, ones, csq[0]); csq[1] = mfma32(f1, ones, csq[1]); }
  }
  {
    LAS float* sc = (LAS float*)(lds + L_X + w * 8192);
    if (g == 0) { sc[r] = cs[0][0]; sc[16 + r] = cs[1][0]; sc[32 + r] = cs[2][0]; sc[48 + r] = cs[3][0]; }
    if (r == 0) { *(LAS f32x4*)(sc + 64 + 4 * g) = csq[0]; *(LAS f32x4*)(sc + 80 + 4 * g) = csq[1]; *(LAS f32x4*)(sc + 96 + 4 * g) = sx; }
    if ((r >> 2) == g) { float d = (r & 3) == 0 ? gx[0] : (r & 3) == 1 ? gx[1] : (r & 3) == 2 ? gx[2] : gx[3]; sc[112 + r] = d; }
  }
  bar();
  float ck[4]; f32x4 cq0 = (f32x4){0, 0, 0, 0}, cq1 = (f32x4){0, 0, 0, 0};
  ck[0] = ck[1] = ck[2] = ck[3] = 0.f;
#pragma unroll
  for (int ww = 0; ww < 8; ++ww) {
    LAS float* sc = (LAS float*)(lds + L_X + ww * 8192);
    ck[0] += sc[r]; ck[1] += sc[16 + r]; ck[2] += sc[32 + r]; ck[3] += sc[48 + r];
    cq0 += *(LAS f32x4*)(sc + 64 + 4 * g); cq1 += *(LAS f32x4*)(sc + 80 + 4 * g);
  }
  f32x4 mean4, rstd4; float mean1, rstd1;
  {
    LAS float* sc = (LAS float*)(lds + L_X + w * 8192);
    f32x4 S = *(LAS f32x4*)(sc + 96 + 4 * g), Q = *(LAS f32x4*)(sc + 112 + 4 * g);
#pragma unroll
    for (int j = 0; j < 4; ++j) { float m = S[j] * (1.f / 512.f); float v = fmaxf(Q[j] * (1.f / 512.f) - m * m, 0.f); mean4[j] = m; rstd4[j] = rsqrtf(v + 1e-5f); }
    float m = sc[96 + r] * (1.f / 512.f); float v = fmaxf(sc[112 + r] * (1.f / 512.f) - m * m, 0.f); mean1 = m; rstd1 = rsqrtf(v + 1e-5f);
  }
  s16x4 ones4; for (int j = 0; j < 4; ++j) ones4[j] = (short)0x3f80;
  f32x4 kvp[2], zp[2];
#pragma unroll
  for (int hd = 0; hd < 2; ++hd) {
    float pk[4], vv[4];
#pragma unroll
    for (int j = 0; j < 4; ++j) {
      pk[j] = phi(rstd4[j] * (acc[hd][j] - mean4[j] * ck[hd]));
      vv[j] = rstd4[j] * (acc[2 + hd][j] - mean4[j] * ck[2 + hd]);
    }
    s16x4 pk4 = pack4(pk[0], pk[1], pk[2], pk[3]), v4 = pack4(vv[0], vv[1], vv[2], vv[3]);
    kvp[hd] = mfma16(pk4, v4, (f32x4){0, 0, 0, 0});
    zp[hd] = mfma16(pk4, ones4, (f32x4){0, 0, 0, 0});
  }
  {
    LAS char* sc = lds + L_X + w * 8192 + 1024;
    *(LAS f32x4*)(sc + lane * 16) = kvp[0];
    *(LAS f32x4*)(sc + 1088 + lane * 16) = kvp[1];
    if (r == 0) { *(LAS f32x4*)(sc + 1024 + g * 16) = zp[0]; *(LAS f32x4*)(sc + 1088 + 1024 + g * 16) = zp[1]; }
  }
  bar();
  if (tid < 136) {
    f32x4 s = (f32x4){0, 0, 0, 0};
#pragma unroll
    for (int ww = 0; ww < 8; ++ww) s += *(LAS f32x4*)(lds + L_X + ww * 8192 + 1024 + tid * 16);
    *(f32x4*)(ws + WS_KVP + (size_t)(id.c * 16 + id.p) * KVP_STRIDE + tid * 16) = s;
  }
  {
    float a[4], b[4];
#pragma unroll
    for (int j = 0; j < 4; ++j) { a[j] = phi(rstd1 * (aq[0][j] - mean1 * cq0[j])); b[j] = phi(rstd1 * (aq[1][j] - mean1 * cq1[j])); }
    u32x4 o; o.x = pk_bf16(a[0], a[1]); o.y = pk_bf16(a[2], a[3]); o.z = pk_bf16(b[0], b[1]); o.w = pk_bf16(b[2], b[3]);
    *(u32x4*)(ws + WS_PQ + ((size_t)((id.p * 8 + w) * 16 + id.c) * 64 + lane) * 16) = o;
  }
}

__global__ __launch_bounds__(512, 2) void k2_o(char* __restrict__ ws) {
  extern __shared__ __attribute__((aligned(16))) char smem_[];
  LAS char* lds = (LAS char*)smem_;
  const int tid = threadIdx.x, lane = tid & 63;
  const int w = __builtin_amdgcn_readfirstlane(tid >> 6);
  const Ids id = ids();
  const int g = lane >> 4, r = lane & 15;
  if (tid < 272) {
    int part = tid >= 136, t = tid - part * 136;
    f32x4 s = (f32x4){0, 0, 0, 0};
#pragma unroll
    for (int i = 0; i < 8; ++i) s += *(const f32x4*)(ws + WS_KVP + (size_t)(id.c * 16 + part * 8 + i) * KVP_STRIDE + t * 16);
    *(LAS f32x4*)(lds + part * 2176 + t * 16) = s;
  }
  u32x4 pq = *(const u32x4*)(ws + WS_PQ + ((size_t)((id.p * 8 + w) * 16 + id.c) * 64 + lane) * 16);
  __syncthreads();
  u32x4 o;
#pragma unroll
  for (int hd = 0; hd < 2; ++hd) {
    f32x4 kv = *(LAS f32x4*)(lds + hd * 1088 + lane * 16) + *(LAS f32x4*)(lds + 2176 + hd * 1088 + lane * 16);
    f32x4 z = *(LAS f32x4*)(lds + hd * 1088 + 1024 + g * 16) + *(LAS f32x4*)(lds + 2176 + hd * 1088 + 1024 + g * 16);
    s16x4 kvA = pack4(kv[0], kv[1], kv[2], kv[3]), zA = pack4(z[0], z[1], z[2], z[3]);
    u32x2 pu; pu.x = hd ? pq.z : pq.x; pu.y = hd ? pq.w : pq.y;
    s16x4 p4 = __builtin_bit_cast(s16x4, pu);
    f32x4 num = mfma16(kvA, p4, (f32x4){0, 0, 0, 0});
    f32x4 den = mfma16(zA, p4, (f32x4){0, 0, 0, 0});
    float o0 = num[0] / (den[0] + 1e-6f), o1 = num[1] / (den[1] + 1e-6f), o2 = num[2] / (den[2] + 1e-6f), o3 = num[3] / (den[3] + 1e-6f);
    if (hd == 0) { o.x = pk_bf16(o0, o1); o.y = pk_bf16(o2, o3); } else { o.z = pk_bf16(o0, o1); o.w = pk_bf16(o2, o3); }
  }
  (void)r;
  *(u32x4*)(ws + WS_O + ((size_t)((id.p * 8 + w) * 16 + id.c) * 64 + lane) * 16) = o;
}

DI unsigned g64_rd(int g, int q, int pp, int u, int b) {
  int rho = 4 * g + q;
  return (unsigned)(128 * rho + 16 * ((2 * pp + u) ^ ((rho >> 1) & 3)) + 8 * (b ^ (pp & 1)));
}

__global__ __launch_bounds__(512, 2) void k3_wo(const bf16_t* __restrict__ x, const bf16_t* __restrict__ wo, char* __restrict__ ws) {
  extern __shared__ __attribute__((aligned(16))) char smem_[];
  LAS char* lds = (LAS char*)smem_;
  const int tid = threadIdx.x, lane = tid & 63;
  const int w = __builtin_amdgcn_readfirstlane(tid >> 6);
  const Ids id = ids();
  const int g = lane >> 4, r = lane & 15, q = (lane >> 2) & 3, pp = lane & 3;
  const unsigned ldsb = (unsigned)(size_t)smem_;
  const int ct = id.c & 7, th = id.c >> 3;
  const int m = w >> 1, kh = w & 1;
  {
    const int row = lane >> 3;
    const unsigned off = (unsigned)(row * 1024 + ct * 128 + (((lane & 7) ^ ((row >> 1) & 3)) * 16));
#pragma unroll
    for (int i = 0; i < 8; ++i) { int blk = w * 8 + i; glds16(wo, off + blk * 8192, ldsb + blk * 1024); }
  }
  const int tile = id.p * 8 + th * 4 + m;
  u32x4 of[8];
#pragma unroll
  for (int i = 0; i < 8; ++i) of[i] = *(const u32x4*)(ws + WS_O + ((size_t)(tile * 16 + kh * 8 + i) * 64 + lane) * 16);
  const u32x4 xr = *(const u32x4*)(x + (size_t)(tile * 16 + r) * 512 + ct * 64 + 16 * g + 8 * kh);
  WAIT_VM(0);
  bar();
  f32x4 acc[4];
#pragma unroll
  for (int t = 0; t < 4; ++t) acc[t] = (f32x4){0, 0, 0, 0};
#pragma unroll
  for (int i = 0; i < 8; ++i) {
    const unsigned base = (unsigned)((kh * 8 + i) * 4096);
#pragma unroll
    for (int t = 0; t < 4; ++t) {
      unsigned a = base + g64_rd(g, q, pp, t >> 1, t & 1);
      s16x8 f = tr_frag(lds, a, a + 2048);
      acc[t] = mfma32(f, __builtin_bit_cast(s16x8, of[i]), acc[t]);
    }
  }
  bar();
  {
    f32x4 s0 = kh ? acc[0] : acc[2], s1 = kh ? acc[1] : acc[3];
    *(LAS f32x4*)(lds + w * 2048 + lane * 16) = s0;
    *(LAS f32x4*)(lds + w * 2048 + 1024 + lane * 16) = s1;
  }
  bar();
  f32x4 k0 = kh ? acc[2] : acc[0], k1 = kh ? acc[3] : acc[1];
  k0 += *(LAS f32x4*)(lds + (w ^ 1) * 2048 + lane * 16);
  k1 += *(LAS f32x4*)(lds + (w ^ 1) * 2048 + 1024 + lane * 16);
  const f32x4 A = (g & 1) ? k1 : k0, B = (g & 1) ? k0 : k1;
  u32x4 hb;
  hb.x = pk_bf16(lo_f(xr.x) + A[0], hi_f(xr.x) + A[1]); hb.y = pk_bf16(lo_f(xr.y) + A[2], hi_f(xr.y) + A[3]);
  hb.z = pk_bf16(lo_f(xr.z) + B[0], hi_f(xr.z) + B[1]); hb.w = pk_bf16(lo_f(xr.w) + B[2], hi_f(xr.w) + B[3]);
  float hv[8] = {lo_f(hb.x), hi_f(hb.x), lo_f(hb.y), hi_f(hb.y), lo_f(hb.z), hi_f(hb.z), lo_f(hb.w), hi_f(hb.w)};
  float s = 0.f;
#pragma unroll
  for (int j = 0; j < 8; ++j) s += hv[j];
  s += __shfl_xor(s, 16); s += __shfl_xor(s, 32);
  const float mean = s * (1.f / 32.f);
  float m2 = 0.f;
#pragma unroll
  for (int j = 0; j < 8; ++j) { float d = hv[j] - mean; m2 += d * d; }
  m2 += __shfl_xor(m2, 16); m2 += __shfl_xor(m2, 32);
  *(u32x4*)(ws + WS_H + ((size_t)(tile * 16 + 2 * ct + kh) * 64 + lane) * 16) = hb;
  if (g == 0) *(f32x2*)(ws + WS_HST + ((size_t)(tile * 16 + 2 * ct + kh) * 16 + r) * 8) = (f32x2){mean, m2};
}

DI float gelu_tanh(float v) {
  float t = v * v;
  float a = v * (-2.3022082f - 0.1029432397f * t);
  float e = __builtin_amdgcn_exp2f(a);
  return v * __builtin_amdgcn_rcpf(1.f + e);
}
__global__ __launch_bounds__(512, 2) void k4_w1(const bf16_t* __restrict__ w1, const bf16_t* __restrict__ b1, char* __restrict__ ws) {
  extern __shared__ __attribute__((aligned(16))) char smem_[];
  LAS char* lds = (LAS char*)smem_;
  const int tid = threadIdx.x, lane = tid & 63;
  const int w = __builtin_amdgcn_readfirstlane(tid >> 6);
  const Ids id = ids();
  const int g = lane >> 4, r = lane & 15, q = (lane >> 2) & 3, pp = lane & 3;
  const unsigned ldsb = (unsigned)(size_t)smem_;
  const int m = w >> 1, kh = w & 1;
  {
    auto srow = [&](int kb, int rho) { return 64 * (kb >> 1) + 16 * ((rho >> 2) & 3) + 8 * (kb & 1) + 4 * (rho >> 4) + (rho & 3); };
#pragma unroll
    for (int i = 0; i < 2; ++i) {
      const int kb = w * 2 + i;
      const unsigned dst = ldsb + kb * 7168;
#pragma unroll
      for (int b = 0; b < 4; ++b) { int rho = b * 8 + (lane >> 3); glds16(w1, (unsigned)(srow(kb, rho) * 3584 + id.c * 224 + (((lane & 7) ^ ((rho >> 1) & 3)) * 16)), dst + b * 1024); }
#pragma unroll
      for (int b = 0; b < 2; ++b) { int rho = b * 16 + (lane >> 2); glds16(w1, (unsigned)(srow(kb, rho) * 3584 + id.c * 224 + 128 + (((lane & 3) ^ ((rho >> 2) & 1)) * 16)), dst + 4096 + b * 1024); }
      { int rho = lane >> 1; glds16(w1, (unsigned)(srow(kb, rho) * 3584 + id.c * 224 + 192 + (lane & 1) * 16), dst + 6144); }
    }
  }
  const int tile0 = id.p * 8 + 2 * m;
  u32x4 hf[2][8];
#pragma unroll
  for (int i = 0; i < 8; ++i)
#pragma unroll
    for (int tt = 0; tt < 2; ++tt) hf[tt][i] = *(const u32x4*)(ws + WS_H + ((size_t)((tile0 + tt) * 16 + kh * 8 + i) * 64 + lane) * 16);
  const int tilef = tile0 + kh;
  f32x2 st[4];
#pragma unroll
  for (int i = 0; i < 4; ++i) st[i] = *(const f32x2*)(ws + WS_HST + ((size_t)(tilef * 16 + 4 * g + i) * 16 + r) * 8);
  const u32x4 bA = *(const u32x4*)(b1 + id.c * 112 + 16 * g), bB = *(const u32x4*)(b1 + id.c * 112 + 16 * g + 8);
  const u32x4 bC = *(const u32x4*)(b1 + id.c * 112 + 64 + 8 * g);
  const u32x2 bD = *(const u32x2*)(b1 + id.c * 112 + 96 + 4 * g);
  WAIT_VM(0);
  bar();
  s16x8 ones; for (int j = 0; j < 8; ++j) ones[j] = (short)0x3f80;
  f32x4 acc[2][7], cs[7];
#pragma unroll
  for (int t = 0; t < 7; ++t) { acc[0][t] = acc[1][t] = cs[t] = (f32x4){0, 0, 0, 0}; }
  const int rho0 = 4 * g + q;
  unsigned rd[7];
#pragma unroll
  for (int t = 0; t < 4; ++t) rd[t] = g64_rd(g, q, pp, t >> 1, t & 1);
#pragma unroll
  for (int b = 0; b < 2; ++b) rd[4 + b] = 4096u + (unsigned)(64 * rho0 + 16 * (pp ^ ((rho0 >> 2) & 1)) + 8 * (b ^ (pp & 1)));
  rd[6] = 6144u + (unsigned)(32 * rho0 + 8 * pp);
#pragma unroll
  for (int i = 0; i < 2; ++i) {
    const unsigned base = (unsigned)((w * 2 + i) * 7168);
#pragma unroll
    for (int t = 0; t < 7; ++t) {
      const unsigned a = base + rd[t];
      s16x8 f = tr_frag(lds, a, a + (t < 4 ? 2048 : t < 6 ? 1024 : 512));
      cs[t] = mfma32(f, ones, cs[t]);
    }
  }
  if (r == 0) {
#pragma unroll
    for (int t = 0; t < 7; ++t) *(LAS f32x4*)(lds + 114688 + w * 448 + t * 64 + g * 16) = cs[t];
  }
#pragma unroll
  for (int i = 0; i < 8; ++i) {
    const unsigned base = (unsigned)((kh * 8 + i) * 7168);
#pragma unroll
    for (int t = 0; t < 7; ++t) {
      const unsigned a = base + rd[t];
      s16x8 f = tr_frag(lds, a, a + (t < 4 ? 2048 : t < 6 ? 1024 : 512));
      acc[0][t] = mfma32(f, __builtin_bit_cast(s16x8, hf[0][i]), acc[0][t]);
      acc[1][t] = mfma32(f, __builtin_bit_cast(s16x8, hf[1][i]), acc[1][t]);
    }
  }
  bar();
#pragma unroll
  for (int t = 0; t < 7; ++t) *(LAS f32x4*)(lds + w * 7168 + t * 1024 + lane * 16) = kh ? acc[0][t] : acc[1][t];
  bar();
  f32x4 v[7];
#pragma unroll
  for (int t = 0; t < 7; ++t) {
    v[t] = (kh ? acc[1][t] : acc[0][t]) + *(LAS f32x4*)(lds + (w ^ 1) * 7168 + t * 1024 + lane * 16);
    f32x4 c = (f32x4){0, 0, 0, 0};
#pragma unroll
    for (int ww = 0; ww < 8; ++ww) c += *(LAS f32x4*)(lds + 114688 + ww * 448 + t * 64 + g * 16);
    cs[t] = c;
  }
  float ms = (st[0].x + st[1].x) + (st[2].x + st[3].x);
  ms += __shfl_xor(ms, 16); ms += __shfl_xor(ms, 32);
  const float mean = ms * (1.f / 16.f);
  float m2 = 0.f;
#pragma unroll
  for (int i = 0; i < 4; ++i) { float d = st[i].x - mean; m2 += st[i].y + 32.f * d * d; }
  m2 += __shfl_xor(m2, 16); m2 += __shfl_xor(m2, 32);
  const float var = m2 * (1.f / 512.f);
  const float r1 = rsqrtf(var + 1e-5f);
  const float rr = r1 * rsqrtf(var * r1 * r1 + 1e-5f);
#pragma unroll
  for (int t = 0; t < 7; ++t) v[t] = (v[t] - mean * cs[t]) * rr;
  char* dst = ws + WS_HH + (size_t)(tilef * 16 + id.c) * 3584;
  const bool od = g & 1;
#pragma unroll
  for (int u = 0; u < 3; ++u) {
    const f32x4 A = od ? v[2 * u + 1] : v[2 * u], B = od ? v[2 * u] : v[2 * u + 1];
    const u32x4 bb = u == 0 ? bA : u == 1 ? bB : bC;
    u32x4 o;
    o.x = pk_bf16(gelu_tanh(A[0] + lo_f(bb.x)), gelu_tanh(A[1] + hi_f(bb.x)));
    o.y = pk_bf16(gelu_tanh(A[2] + lo_f(bb.y)), gelu_tanh(A[3] + hi_f(bb.y)));
    o.z = pk_bf16(gelu_tanh(B[0] + lo_f(bb.z)), gelu_tanh(B[1] + hi_f(bb.z)));
    o.w = pk_bf16(gelu_tanh(B[2] + lo_f(bb.w)), gelu_tanh(B[3] + hi_f(bb.w)));
    *(u32x4*)(dst + u * 1024 + lane * 16) = o;
  }
  {
    u32x2 o;
    o.x = pk_bf16(gelu_tanh(v[6][0] + lo_f(bD.x)), gelu_tanh(v[6][1] + hi_f(bD.x)));
    o.y = pk_bf16(gelu_tanh(v[6][2] + lo_f(bD.y)), gelu_tanh(v[6][3] + hi_f(bD.y)));
    *(u32x2*)(dst + 3072 + lane * 8) = o;
  }
}

DI int w2_row(int kq, int i, int rho) {
  const int gg = (rho >> 2) & 3, j = 4 * (rho >> 4) + (rho & 3);
  if (i < 12) { int cp = 4 * kq + i / 3, u = i % 3; return u < 2 ? 112 * cp + 16 * gg + 8 * u + j : 112 * cp + 64 + 8 * gg + j; }
  int cp = 4 * kq + 2 * (i - 12) + (j >> 2);
  return 112 * cp + 96 + 4 * gg + (j & 3);
}
__global__ __launch_bounds__(512, 2) void k5_w2(const bf16_t* __restrict__ w2, const bf16_t* __restrict__ b2, char* __restrict__ ws, bf16_t* __restrict__ out) {
  extern __shared__ __attribute__((aligned(16))) char smem_[];
  LAS char* lds = (LAS char*)smem_;
  const int tid = threadIdx.x, lane = tid & 63;
  const int w = __builtin_amdgcn_readfirstlane(tid >> 6);
  const Ids id = ids();
  const int g = lane >> 4, r = lane & 15, q = (lane >> 2) & 3, pp = lane & 3;
  const unsigned ldsb = (unsigned)(size_t)smem_;
  const int ct = id.c & 7, th = id.c >> 3;
  const int m = w >> 2, kq = w & 3;
  auto issue_w2 = [&](int rd) {
#pragma unroll
    for (int n = 0; n < 14; ++n) {
      int blk = (w >> 2) * 14 + n;
      int il = blk >> 2, b = blk & 3;
      int rho = b * 8 + (lane >> 3);
      glds16(w2, (unsigned)(w2_row(kq, rd * 7 + il, rho) * 1024 + ct * 128 + (((lane & 7) ^ ((rho >> 1) & 3)) * 16)), ldsb + (kq * 7 + il) * 4096 + b * 1024);
    }
  };
  issue_w2(0);
  const int tile0 = id.p * 8 + th * 4 + 2 * m;
  u32x4 af[2][14];
#pragma unroll
  for (int i = 0; i < 14; ++i)
#pragma unroll
    for (int tt = 0; tt < 2; ++tt) {
      const char* src = ws + WS_HH + (size_t)((tile0 + tt) * 16 + 4 * kq) * 3584;
      if (i < 12) af[tt][i] = *(const u32x4*)(src + (i / 3) * 3584 + (i % 3) * 1024 + lane * 16);
      else {
        u32x2 a = *(const u32x2*)(src + (2 * (i - 12)) * 3584 + 3072 + lane * 8), b = *(const u32x2*)(src + (2 * (i - 12) + 1) * 3584 + 3072 + lane * 8);
        af[tt][i] = (u32x4){a.x, a.y, b.x, b.y};
      }
    }
  const int tilef = tile0 + (kq >> 1), kh = kq & 1;
  const u32x4 hb = *(const u32x4*)(ws + WS_H + ((size_t)(tilef * 16 + 2 * ct + kh) * 64 + lane) * 16);
  const u32x4 bb = *(const u32x4*)(b2 + ct * 64 + 16 * g + 8 * kh);
  f32x4 acc[2][4];
#pragma unroll
  for (int t = 0; t < 4; ++t) acc[0][t] = acc[1][t] = (f32x4){0, 0, 0, 0};
  unsigned rd4[4];
#pragma unroll
  for (int t = 0; t < 4; ++t) rd4[t] = g64_rd(g, q, pp, t >> 1, t & 1);
#pragma unroll
  for (int rd = 0; rd < 2; ++rd) {
    WAIT_VM(0);
    bar();
#pragma unroll
    for (int il = 0; il < 7; ++il) {
      const unsigned base = (unsigned)((kq * 7 + il) * 4096);
#pragma unroll
      for (int t = 0; t < 4; ++t) {
        s16x8 f = tr_frag(lds, base + rd4[t], base + rd4[t] + 2048);
        acc[0][t] = mfma32(f, __builtin_bit_cast(s16x8, af[0][rd * 7 + il]), acc[0][t]);
        acc[1][t] = mfma32(f, __builtin_bit_cast(s16x8, af[1][rd * 7 + il]), acc[1][t]);
      }
    }
    bar();
    if (rd == 0) issue_w2(1);
  }
#pragma unroll
  for (int tt = 0; tt < 2; ++tt)
#pragma unroll
    for (int t = 0; t < 4; ++t) *(LAS f32x4*)(lds + w * 8192 + (tt * 4 + t) * 1024 + lane * 16) = acc[tt][t];
  bar();
  f32x4 k0 = (f32x4){0, 0, 0, 0}, k1 = (f32x4){0, 0, 0, 0};
#pragma unroll
  for (int s = 0; s < 4; ++s) {
    k0 += *(LAS f32x4*)(lds + (m * 4 + s) * 8192 + ((kq >> 1) * 4 + 2 * kh) * 1024 + lane * 16);
    k1 += *(LAS f32x4*)(lds + (m * 4 + s) * 8192 + ((kq >> 1) * 4 + 2 * kh + 1) * 1024 + lane * 16);
  }
  const f32x4 A = (g & 1) ? k1 : k0, B = (g & 1) ? k0 : k1;
  u32x4 o;
  o.x = pk_bf16(lo_f(hb.x) + A[0] + lo_f(bb.x), hi_f(hb.x) + A[1] + hi_f(bb.x));
  o.y = pk_bf16(lo_f(hb.y) + A[2] + lo_f(bb.y), hi_f(hb.y) + A[3] + hi_f(bb.y));
  o.z = pk_bf16(lo_f(hb.z) + B[0] + lo_f(bb.z), hi_f(hb.z) + B[1] + hi_f(bb.z));
  o.w = pk_bf16(lo_f(hb.w) + B[2] + lo_f(bb.w), hi_f(hb.w) + B[3] + hi_f(bb.w));
  *(u32x4*)(out + (size_t)(tilef * 16 + r) * 512 + ct * 64 + 16 * g + 8 * kh) = o;
}

extern "C" void kernel_launch(void* const* d_in, const int* in_sizes, int n_in, void* d_out, int out_size, void* d_ws, size_t ws_size, hipStream_t stream) {
  const bf16_t* x = (const bf16_t*)d_in[0]; const bf16_t* wqkv = (const bf16_t*)d_in[1]; const bf16_t* wo = (const bf16_t*)d_in[2];
  const bf16_t* w1 = (const bf16_t*)d_in[4]; const bf16_t* b1 = (const bf16_t*)d_in[5]; const bf16_t* w2 = (const bf16_t*)d_in[6]; const bf16_t* b2 = (const bf16_t*)d_in[7];
  char* ws = (char*)d_ws;
  (void)hipFuncSetAttribute((const void*)k1_qkv, hipFuncAttributeMaxDynamicSharedMemorySize, 163840);
  (void)hipFuncSetAttribute((const void*)k3_wo, hipFuncAttributeMaxDynamicSharedMemorySize, 65536);
  (void)hipFuncSetAttribute((const void*)k4_w1, hipFuncAttributeMaxDynamicSharedMemorySize, 118784);
  (void)hipFuncSetAttribute((const void*)k5_w2, hipFuncAttributeMaxDynamicSharedMemorySize, 114688);
  k1_qkv<<<256, 512, 163840, stream>>>(x, wqkv, ws);
  k2_o<<<256, 512, 8192, stream>>>(ws);
  k3_wo<<<256, 512, 65536, stream>>>(x, wo, ws);
  k4_w1<<<256, 512, 118784, stream>>>(w1, b1, ws);
  k5_w2<<<256, 512, 114688, stream>>>(w2, b2, ws, (bf16_t*)d_out);
}
```

```cpp
#include <hip/hip_runtime.h>
#include <stdint.h>

typedef unsigned short bf16_t;
typedef unsigned long long u64;
typedef __attribute__((ext_vector_type(8))) short s16x8;
typedef __attribute__((ext_vector_type(4))) short s16x4;
typedef __attribute__((ext_vector_type(4))) float f32x4;
typedef __attribute__((ext_vector_type(2))) float f32x2;
typedef __attribute__((ext_vector_type(4))) unsigned u32x4;
typedef __attribute__((ext_vector_type(2))) unsigned u32x2;
#define LAS __attribute__((address_space(3)))
#define DI __device__ __forceinline__

constexpr unsigned WS_E    = 0;
constexpr unsigned WS_F1   = 4096;
constexpr unsigned WS_F2   = 4096 + 2048;
constexpr unsigned WS_F3   = 4096 + 4096;
constexpr unsigned WS_F4   = 4096 + 6144;
constexpr unsigned WS_KVP  = 65536;
constexpr unsigned WS_HST  = 655360;
constexpr unsigned WS_O    = 1u << 20;
constexpr unsigned WS_H    = 3u << 20;
constexpr unsigned WS_HH   = 5u << 20;
constexpr int KVP_STRIDE = 2176;

DI unsigned pk_bf16(float lo, float hi) { unsigned r; asm("v_cvt_pk_bf16_f32 %0, %1, %2" : "=v"(r) : "v"(lo), "v"(hi)); return r; }
DI float lo_f(unsigned u) { return __uint_as_float(u << 16); }
DI float hi_f(unsigned u) { return __uint_as_float(u & 0xffff0000u); }

DI void glds16(const void* sbase, unsigned voff, unsigned lds_dst) {
  unsigned keep;
  asm volatile("s_mov_b32 %0, m0\n\ts_mov_b32 m0, %3\n\ts_nop 0\n\tglobal_load_lds_dwordx4 %1, %2\n\ts_mov_b32 m0, %0"
               : "=&s"(keep) : "v"(voff), "s"(sbase), "s"(lds_dst) : "memory");
}
#define WAIT_VM(n) asm volatile("s_waitcnt vmcnt(" #n ")" ::: "memory")
DI void bar() { asm volatile("s_waitcnt lgkmcnt(0)" ::: "memory"); __builtin_amdgcn_s_barrier(); asm volatile("" ::: "memory"); }
#define SCHED_FENCE() __builtin_amdgcn_sched_barrier(0)

DI s16x8 tr_frag(LAS char* lds, unsigned a0, unsigned a1) {
  s16x4 a = __builtin_amdgcn_ds_read_tr16_b64_v4i16((LAS s16x4*)(lds + a0));
  s16x4 b = __builtin_amdgcn_ds_read_tr16_b64_v4i16((LAS s16x4*)(lds + a1));
  return __builtin_shufflevector(a, b, 0, 1, 2, 3, 4, 5, 6, 7);
}
DI f32x4 mfma32(s16x8 a, s16x8 b, f32x4 c) { return __builtin_amdgcn_mfma_f32_16x16x32_bf16(a, b, c, 0, 0, 0); }
DI f32x4 mfma16(s16x4 a, s16x4 b, f32x4 c) { return __builtin_amdgcn_mfma_f32_16x16x16bf16_1k(a, b, c, 0, 0, 0); }
DI float phi(float t) { return (t > 0.f ? t + 1.f : __expf(t)) + 1e-6f; }
DI s16x8 as_frag(u32x4 v) { return __builtin_bit_cast(s16x8, v); }

typedef __amdgpu_buffer_rsrc_t rsrc_t;
#define XAUX 16
DI u32x4 ld16_sc1(rsrc_t rs, unsigned off) { return __builtin_amdgcn_raw_buffer_load_b128(rs, off, 0, 16); }
DI u32x4 ld16_sc1(rsrc_t rs, unsigned off, unsigned soff) { return __builtin_amdgcn_raw_buffer_load_b128(rs, off, soff, XAUX); }
DI u32x2 ld8_sc1(rsrc_t rs, unsigned off, unsigned soff = 0) { return __builtin_amdgcn_raw_buffer_load_b64(rs, off, soff, 16); }
DI void st16_sc1(rsrc_t rs, unsigned off, u32x4 v) { __builtin_amdgcn_raw_buffer_store_b128(v, rs, off, 0, 16); }
DI void st8_sc1(rsrc_t rs, unsigned off, u32x2 v) { __builtin_amdgcn_raw_buffer_store_b64(v, rs, off, 0, 16); }

DI void wait_flags(const char* ws, unsigned off, unsigned tlo, unsigned thi) {
  for (int it = 0; it < (1 << 18); ++it) {
    u32x2 v;
    asm volatile("global_load_dwordx2 %0, %1, %2 sc1\n\ts_waitcnt vmcnt(0)" : "=v"(v) : "v"(off), "s"(ws) : "memory");
    const bool ok = (v.x == tlo) & (v.y == thi);
    if (__builtin_amdgcn_ballot_w64(ok) == ~0ull) break;
    __builtin_amdgcn_s_sleep(1);
  }
  asm volatile("" ::: "memory");
}

#define TS(i)

DI unsigned g64_rd(int g, int q, int pp, int u, int b) {
  int rho = 4 * g + q;
  return (unsigned)(128 * rho + 16 * ((2 * pp + u) ^ ((rho >> 1) & 3)) + 8 * (b ^ (pp & 1)));
}
DI float gelu_tanh(float v) {
  float t = v * v;
  float a = v * (-2.3022082f - 0.1029432397f * t);
  float e = __builtin_amdgcn_exp2f(a);
  return v * __builtin_amdgcn_rcpf(1.f + e);
}
DI int w2_row(int kq, int i, int rho) {
  const int gg = (rho >> 2) & 3, j = 4 * (rho >> 4) + (rho & 3);
  if (i < 12) { int cp = 4 * kq + i / 3, u = i % 3; return u < 2 ? 112 * cp + 16 * gg + 8 * u + j : 112 * cp + 64 + 8 * gg + j; }
  int cp = 4 * kq + 2 * (i - 12) + (j >> 2);
  return 112 * cp + 96 + 4 * gg + (j & 3);
}
DI int slotB(int j) { return j < 7 ? j : j + 7; }
DI int slotC(int j) { return j < 7 ? 7 + j : 14 + j; }
DI int w2_slot(int kq, int il) {
  switch (il) {
    case 0: case 7: return 28 + kq;
    case 1: case 8: return 32 + kq;
    case 2: case 9: return 36 + kq;
    case 3: case 10: return slotB(kq);
    case 4: return slotB(4 + kq);
    case 5: return slotB(8 + kq);
    case 6: return kq < 2 ? slotB(12 + kq) : slotC(10 + kq);
    case 11: return slotC(kq);
    case 12: return slotC(4 + kq);
    default: return slotC(8 + kq);
  }
}

__global__ __launch_bounds__(512, 2) void fused_block(const bf16_t* __restrict__ x, const bf16_t* __restrict__ wqkv, const bf16_t* __restrict__ wo,
                                                      const bf16_t* __restrict__ w1, const bf16_t* __restrict__ b1, const bf16_t* __restrict__ w2,
                                                      const bf16_t* __restrict__ b2, char* ws, bf16_t* __restrict__ out) {
  extern __shared__ __attribute__((aligned(16))) char smem_[];
  LAS char* lds = (LAS char*)smem_;
  const int tid = threadIdx.x, lane = tid & 63;
  const int w = __builtin_amdgcn_readfirstlane(tid >> 6);
  const int bid = blockIdx.x;
  const int P = (bid & 7) * 2 + (bid >> 7), C = (bid >> 3) & 15;
  const int g = lane >> 4, r = lane & 15, q = (lane >> 2) & 3, pp = lane & 3;
  const unsigned ldsb = (unsigned)(size_t)smem_;
  const rsrc_t rs = __builtin_amdgcn_make_buffer_rsrc(ws, 0, 0x7fffffff, 0x00020000);
  TS(0);
  u32x2 Ev;
  asm volatile("global_load_dwordx2 %0, %1, %2 sc1" : "=v"(Ev) : "v"(WS_E), "s"(ws) : "memory");

  s16x8 ones; for (int j = 0; j < 8; ++j) ones[j] = (short)0x3f80;
  const f32x4 zero4 = (f32x4){0, 0, 0, 0};
  const int ct = C & 7, th = C >> 3;

  constexpr unsigned L_WKV = 0, L_WQ = 65536, L_X = 98304;
  const int m1 = w >> 1, n1 = w & 1;
  u32x4 pq;
  unsigned tlo, thi;
  {
    const int xrow = lane >> 3;
    const unsigned xoff = (unsigned)(((P * 128 + w * 16 + xrow) * 512) * 2 + (((lane & 7) ^ (xrow & 7)) * 16));
    const int wk = lane >> 2;
    const unsigned wcol = (unsigned)((((((lane >> 1) & 1) ^ ((wk >> 3) & 1)) * 16) + (lane & 1) * 8) * 2);
    const unsigned woff = (unsigned)(wk * 1536 * 2) + wcol + (unsigned)(C * 64);
    auto issue_x = [&](int ch) {
      unsigned dst = ldsb + L_X + (ch & 3) * 16384 + w * 2048;
      glds16(x, xoff + ch * 128, dst);
      glds16(x, xoff + ch * 128 + 8 * 1024, dst + 1024);
    };
    auto issue_wkv = [&](int ch) {
      int sub = w >> 1, seg = w & 1;
      glds16(wqkv, woff + (unsigned)((ch * 64 + sub * 16) * 3072 + (1 + seg) * 1024), ldsb + L_WKV + ch * 8192 + w * 1024);
    };
    auto issue_wq = [&](int i) {
      int blk = w * 4 + i;
      glds16(wqkv, woff + (unsigned)(blk * 16 * 3072), ldsb + L_WQ + blk * 1024);
    };
#pragma unroll
    for (int ch = 0; ch < 4; ++ch) { issue_x(ch); issue_wkv(ch); }
#pragma unroll
    for (int ch = 4; ch < 8; ++ch) issue_wkv(ch);

    TS(53);
    const unsigned xrd_own = L_X + w * 2048 + r * 128, xrd_oth = L_X + (w ^ 1) * 2048 + r * 128;
    const unsigned xsw0 = (unsigned)(((0 + g) ^ (r & 7)) * 16), xsw1 = (unsigned)(((4 + g) ^ (r & 7)) * 16);
    const unsigned wrd = (unsigned)((g >> 1) * 1024 + (8 * (g & 1) + q) * 64 + 8 * pp);
    const unsigned wrd_n = L_WKV + (g >> 1) * 1024 + wrd + 32u * (n1 ^ (g & 1));

    s16x8 xf[16];
    s16x8 xo[2][2], wf[2][2][2];
    f32x4 acc[2][2];
    f32x4 cs[2], sx = zero4, gx = zero4;
    acc[0][0] = acc[0][1] = acc[1][0] = acc[1][1] = cs[0] = cs[1] = zero4;
#pragma unroll
    for (int ch = 0; ch <= 8; ++ch) {
      if (ch < 8) {
        if (ch == 0) WAIT_VM(13); else if (ch == 1) WAIT_VM(10); else if (ch == 2) WAIT_VM(9); else if (ch == 3) WAIT_VM(8);
        else if (ch == 4) WAIT_VM(4); else if (ch == 5) WAIT_VM(8); else if (ch == 6) WAIT_VM(6); else WAIT_VM(4);
        bar();
        if (ch == 0) TS(45); if (ch == 1) TS(46); if (ch == 2) TS(47); if (ch == 3) TS(48); if (ch == 4) TS(49); if (ch == 5) TS(50); if (ch == 6) TS(51); if (ch == 7) TS(52);
        if (ch >= 1 && ch <= 4) issue_x(ch + 3);
        if (ch == 4) {
#pragma unroll
          for (int i = 0; i < 4; ++i) issue_wq(i);
        }
#pragma unroll
        for (int s = 0; s < 2; ++s) {
          xf[ch * 2 + s] = *(LAS s16x8*)(lds + xrd_own + (ch & 3) * 16384 + (s ? xsw1 : xsw0));
          xo[ch & 1][s] = *(LAS s16x8*)(lds + xrd_oth + (ch & 3) * 16384 + (s ? xsw1 : xsw0));
#pragma unroll
          for (int seg = 0; seg < 2; ++seg) {
            const unsigned a = wrd_n + ch * 8192 + s * 4096 + seg * 1024;
            wf[ch & 1][s][seg] = tr_frag(lds, a, a + 256);
          }
        }
        SCHED_FENCE();
      }
      if (ch > 0) {
        const int c0 = ch - 1;
#pragma unroll
        for (int s = 0; s < 2; ++s) {
          const s16x8 xa = xf[c0 * 2 + s], xb = xo[c0 & 1][s];
          acc[0][0] = mfma32(xa, wf[c0 & 1][s][0], acc[0][0]);
          acc[0][1] = mfma32(xa, wf[c0 & 1][s][1], acc[0][1]);
          acc[1][0] = mfma32(xb, wf[c0 & 1][s][0], acc[1][0]);
          acc[1][1] = mfma32(xb, wf[c0 & 1][s][1], acc[1][1]);
          sx = mfma32(xa, ones, sx);
          gx = mfma32(xa, xa, gx);
        }
        if ((c0 >> 1) == m1) {
#pragma unroll
          for (int s = 0; s < 2; ++s) {
            cs[0] = mfma32(ones, wf[c0 & 1][s][0], cs[0]);
            cs[1] = mfma32(ones, wf[c0 & 1][s][1], cs[1]);
          }
        }
        SCHED_FENCE();
      }
    }
    TS(1);
    {
      LAS float* sc = (LAS float*)(lds + w * 256);
      if (g == 0) { sc[r] = cs[0][0]; sc[16 + r] = cs[1][0]; }
      if (r == 0) *(LAS f32x4*)(sc + 32 + 4 * g) = sx;
      if ((r >> 2) == g) { float d = (r & 3) == 0 ? gx[0] : (r & 3) == 1 ? gx[1] : (r & 3) == 2 ? gx[2] : gx[3]; sc[48 + r] = d; }
    }
    bar();
    TS(20);
    if (w >= 4) {
      const int row = lane >> 3;
      const unsigned off = (unsigned)(row * 1024 + ct * 128 + (((lane & 7) ^ ((row >> 1) & 3)) * 16));
#pragma unroll
      for (int i = 0; i < 16; ++i) { int blk = (w - 4) * 16 + i; glds16(wo, off + blk * 8192, ldsb + L_X + blk * 1024); }
    }
    TS(21);
    float mean1, rstd1;
    {
      float ck = 0.f, cv = 0.f;
#pragma unroll
      for (int mm = 0; mm < 4; ++mm) { LAS float* sc = (LAS float*)(lds + (2 * mm + n1) * 256); ck += sc[r]; cv += sc[16 + r]; }
      float pk[8], vv[8];
#pragma unroll
      for (int t = 0; t < 2; ++t) {
        LAS float* sc = (LAS float*)(lds + (w ^ t) * 256);
        f32x4 S = *(LAS f32x4*)(sc + 32 + 4 * g), Q = *(LAS f32x4*)(sc + 48 + 4 * g);
#pragma unroll
        for (int j = 0; j < 4; ++j) {
          float mu = S[j] * (1.f / 512.f); float var = fmaxf(Q[j] * (1.f / 512.f) - mu * mu, 0.f); float rsd = rsqrtf(var + 1e-5f);
          pk[t * 4 + j] = phi(rsd * (acc[t][0][j] - mu * ck));
          vv[t * 4 + j] = rsd * (acc[t][1][j] - mu * cv);
        }
      }
      {
        LAS float* sc = (LAS float*)(lds + w * 256);
        float mu = sc[32 + r] * (1.f / 512.f); float var = fmaxf(sc[48 + r] * (1.f / 512.f) - mu * mu, 0.f); mean1 = mu; rstd1 = rsqrtf(var + 1e-5f);
      }
      u32x4 pk8, v8;
      pk8.x = pk_bf16(pk[0], pk[1]); pk8.y = pk_bf16(pk[2], pk[3]); pk8.z = pk_bf16(pk[4], pk[5]); pk8.w = pk_bf16(pk[6], pk[7]);
      v8.x = pk_bf16(vv[0], vv[1]); v8.y = pk_bf16(vv[2], vv[3]); v8.z = pk_bf16(vv[4], vv[5]); v8.w = pk_bf16(vv[6], vv[7]);
      f32x4 kvp = mfma32(as_frag(pk8), as_frag(v8), zero4);
      f32x4 zp = mfma32(as_frag(pk8), ones, zero4);
      LAS char* sc = lds + 4096 + w * 1088;
      *(LAS f32x4*)(sc + lane * 16) = kvp;
      if (r == 0) *(LAS f32x4*)(sc + 1024 + g * 16) = zp;
    }
    TS(22);
    if (w >= 4) WAIT_VM(16); else WAIT_VM(0);
    TS(23);
    bar();
    TS(24);
    asm volatile("" : "+v"(Ev));
    {
      const u64 E = ((u64)(unsigned)__builtin_amdgcn_readfirstlane(Ev.y) << 32) | (unsigned)__builtin_amdgcn_readfirstlane(Ev.x);
      const u64 T = E * 0x9E3779B97F4A7C15ull + 0xD1B54A32D192ED03ull;
      tlo = (unsigned)T; thi = (unsigned)(T >> 32);
    }
    if (w == 0) {
#pragma unroll
      for (int rd = 0; rd < 3; ++rd) {
        const int i = rd * 64 + lane;
        if (i < 136) {
          const int hd = i >= 68, wi = i - hd * 68;
          f32x4 s = zero4;
#pragma unroll
          for (int mm = 0; mm < 4; ++mm) s += *(LAS f32x4*)(lds + 4096 + (2 * mm + hd) * 1088 + wi * 16);
          st16_sc1(rs, WS_KVP + (unsigned)((C * 16 + P) * KVP_STRIDE + i * 16), __builtin_bit_cast(u32x4, s));
        }
      }
    }
    TS(2);
    f32x4 aq[2], csq[2];
    aq[0] = aq[1] = csq[0] = csq[1] = zero4;
    {
      const unsigned a0 = L_WQ + wrd + 32u * (0 ^ (g & 1)), a1 = L_WQ + wrd + 32u * (1 ^ (g & 1));
      s16x8 f[2][4];
#pragma unroll
      for (int kk = 0; kk <= 8; ++kk) {
        if (kk < 8) {
#pragma unroll
          for (int s = 0; s < 2; ++s) {
            f[kk & 1][s * 2 + 0] = tr_frag(lds, a0 + (kk * 2 + s) * 2048, a0 + (kk * 2 + s) * 2048 + 256);
            f[kk & 1][s * 2 + 1] = tr_frag(lds, a1 + (kk * 2 + s) * 2048, a1 + (kk * 2 + s) * 2048 + 256);
          }
          SCHED_FENCE();
        }
        if (kk > 0) {
          const int k0 = kk - 1;
#pragma unroll
          for (int s = 0; s < 2; ++s) {
            aq[0] = mfma32(f[k0 & 1][s * 2 + 0], xf[k0 * 2 + s], aq[0]);
            aq[1] = mfma32(f[k0 & 1][s * 2 + 1], xf[k0 * 2 + s], aq[1]);
            csq[0] = mfma32(f[k0 & 1][s * 2 + 0], ones, csq[0]);
            csq[1] = mfma32(f[k0 & 1][s * 2 + 1], ones, csq[1]);
          }
          SCHED_FENCE();
        }
      }
    }
    TS(25);
    if (w == 0) {
      WAIT_VM(0);
      if (lane == 0) st8_sc1(rs, WS_F1 + (unsigned)((C * 16 + P) * 8), (u32x2){tlo, thi});
    }
    {
      float a[4], b[4];
#pragma unroll
      for (int j = 0; j < 4; ++j) { a[j] = phi(rstd1 * (aq[0][j] - mean1 * csq[0][j])); b[j] = phi(rstd1 * (aq[1][j] - mean1 * csq[1][j])); }
      pq.x = pk_bf16(a[0], a[1]); pq.y = pk_bf16(a[2], a[3]); pq.z = pk_bf16(b[0], b[1]); pq.w = pk_bf16(b[2], b[3]);
    }
  }
  bar();
  TS(3);
  auto issue_w1 = [&](int kb) {
    auto srow = [&](int rho) { return 64 * (kb >> 1) + 16 * ((rho >> 2) & 3) + 8 * (kb & 1) + 4 * (rho >> 4) + (rho & 3); };
    const unsigned dst = ldsb + kb * 7168;
#pragma unroll
    for (int b = 0; b < 4; ++b) { int rho = b * 8 + (lane >> 3); glds16(w1, (unsigned)(srow(rho) * 3584 + C * 224 + (((lane & 7) ^ ((rho >> 1) & 3)) * 16)), dst + b * 1024); }
#pragma unroll
    for (int b = 0; b < 2; ++b) { int rho = b * 16 + (lane >> 2); glds16(w1, (unsigned)(srow(rho) * 3584 + C * 224 + 128 + (((lane & 3) ^ ((rho >> 2) & 1)) * 16)), dst + 4096 + b * 1024); }
    { int rho = lane >> 1; glds16(w1, (unsigned)(srow(rho) * 3584 + C * 224 + 192 + (lane & 1) * 16), dst + 6144); }
  };
  issue_w1(w);
  const int m2 = w >> 2, kq = w & 3;
  unsigned rd4[4];
#pragma unroll
  for (int t = 0; t < 4; ++t) rd4[t] = g64_rd(g, q, pp, t >> 1, t & 1);
  TS(26);
  WAIT_VM(7);
  TS(27);
  bar();
  TS(28);
  s16x8 wof[4][4];
#pragma unroll
  for (int i = 0; i < 4; ++i)
#pragma unroll
    for (int t = 0; t < 4; ++t) { const unsigned a = L_X + (unsigned)((kq * 4 + i) * 4096) + rd4[t]; wof[i][t] = tr_frag(lds, a, a + 2048); }
  bar();
  TS(4);
  constexpr unsigned L_SP = 114688;
  wait_flags(ws, WS_F1 + (unsigned)((C * 16 + 2 * w + (lane & 1)) * 8), tlo, thi);
  TS(5);
  {
    const unsigned base = WS_KVP + (unsigned)((C * 16 + 2 * w) * KVP_STRIDE);
    u32x4 a[3], b[3];
#pragma unroll
    for (int rd = 0; rd < 3; ++rd) {
      const int i = rd < 2 ? rd * 64 + lane : 128 + (lane & 7);
      a[rd] = ld16_sc1(rs, base + i * 16);
      b[rd] = ld16_sc1(rs, base + KVP_STRIDE + i * 16);
    }
#pragma unroll
    for (int rd = 0; rd < 3; ++rd) {
      const int i = rd < 2 ? rd * 64 + lane : 128 + (lane & 7);
      *(LAS f32x4*)(lds + L_SP + w * 2176 + i * 16) = __builtin_bit_cast(f32x4, a[rd]) + __builtin_bit_cast(f32x4, b[rd]);
    }
  }
  TS(29);
  bar();
  if (tid < 136) {
    f32x4 s = zero4;
#pragma unroll
    for (int ww = 0; ww < 8; ++ww) s += *(LAS f32x4*)(lds + L_SP + ww * 2176 + tid * 16);
    *(LAS u32x2*)(lds + L_SP + 17408 + tid * 8) = (u32x2){pk_bf16(s[0], s[1]), pk_bf16(s[2], s[3])};
  }
  bar();
  {
    u32x4 o;
#pragma unroll
    for (int hd = 0; hd < 2; ++hd) {
      s16x4 kvA = *(LAS s16x4*)(lds + L_SP + 17408 + (hd * 68 + lane) * 8);
      s16x4 zA = *(LAS s16x4*)(lds + L_SP + 17408 + (hd * 68 + 64 + g) * 8);
      u32x2 pu; pu.x = hd ? pq.z : pq.x; pu.y = hd ? pq.w : pq.y;
      s16x4 p4 = __builtin_bit_cast(s16x4, pu);
      f32x4 num = mfma16(kvA, p4, zero4);
      f32x4 den = mfma16(zA, p4, zero4);
      const float inv = 1.f / (den[0] + 1e-6f);
      if (hd == 0) { o.x = pk_bf16(num[0] * inv, num[1] * inv); o.y = pk_bf16(num[2] * inv, num[3] * inv); }
      else { o.z = pk_bf16(num[0] * inv, num[1] * inv); o.w = pk_bf16(num[2] * inv, num[3] * inv); }
    }
    st16_sc1(rs, WS_O + (unsigned)((((P * 8 + w) * 16 + C) * 64 + lane) * 16), o);
  }
  TS(30);
  WAIT_VM(0);
  TS(31);
  bar();
  if (tid == 0) st8_sc1(rs, WS_F2 + (unsigned)((P * 16 + C) * 8), (u32x2){tlo, thi});
  TS(6);
  issue_w1(8 + w);
  const int tile0 = P * 8 + th * 4 + 2 * m2;
  const int tilef = tile0 + (kq >> 1), khf = kq & 1;
  unsigned xo_off = (unsigned)(((tilef * 16 + r) * 512 + ct * 64 + 16 * g + 8 * khf) * 2);
  const u32x4 xr = *(const u32x4*)((const char*)x + xo_off);
  wait_flags(ws, WS_F2 + (unsigned)((P * 16 + 4 * kq + (lane & 3)) * 8), tlo, thi);
  TS(7);
  u32x4 hb;
  {
    u32x4 of[2][4];
#pragma unroll
    for (int i = 0; i < 4; ++i)
#pragma unroll
      for (int tt = 0; tt < 2; ++tt) of[tt][i] = ld16_sc1(rs, (unsigned)(lane * 16 + i * 1024), WS_O + (unsigned)(((tile0 + tt) * 16 + kq * 4) * 1024));
    SCHED_FENCE();
    f32x4 acc[2][4];
#pragma unroll
    for (int t = 0; t < 4; ++t) acc[0][t] = acc[1][t] = zero4;
#pragma unroll
    for (int i = 0; i < 4; ++i)
#pragma unroll
      for (int tt = 0; tt < 2; ++tt)
#pragma unroll
        for (int t = 0; t < 4; ++t) acc[tt][t] = mfma32(wof[i][t], as_frag(of[tt][i]), acc[tt][t]);
    f32x4 k0 = zero4, k1 = zero4;
#pragma unroll
    for (int d = 0; d < 4; ++d) {
      if (d == kq) { k0 = acc[d >> 1][2 * (d & 1)]; k1 = acc[d >> 1][2 * (d & 1) + 1]; }
      else {
        LAS char* dst = lds + L_SP + ((m2 * 4 + d) * 3 + ((kq - d - 1) & 3)) * 2048 + lane * 16;
        *(LAS f32x4*)dst = acc[d >> 1][2 * (d & 1)];
        *(LAS f32x4*)(dst + 1024) = acc[d >> 1][2 * (d & 1) + 1];
      }
    }
    TS(32);
    bar();
    TS(33);
#pragma unroll
    for (int s = 0; s < 3; ++s) {
      k0 += *(LAS f32x4*)(lds + L_SP + (w * 3 + s) * 2048 + lane * 16);
      k1 += *(LAS f32x4*)(lds + L_SP + (w * 3 + s) * 2048 + 1024 + lane * 16);
    }
    const f32x4 A = (g & 1) ? k1 : k0, B = (g & 1) ? k0 : k1;
    hb.x = pk_bf16(lo_f(xr.x) + A[0], hi_f(xr.x) + A[1]); hb.y = pk_bf16(lo_f(xr.y) + A[2], hi_f(xr.y) + A[3]);
    hb.z = pk_bf16(lo_f(xr.z) + B[0], hi_f(xr.z) + B[1]); hb.w = pk_bf16(lo_f(xr.w) + B[2], hi_f(xr.w) + B[3]);
    st16_sc1(rs, WS_H + (unsigned)(((tilef * 16 + 2 * ct + khf) * 64 + lane) * 16), hb);
    float hv[8] = {lo_f(hb.x), hi_f(hb.x), lo_f(hb.y), hi_f(hb.y), lo_f(hb.z), hi_f(hb.z), lo_f(hb.w), hi_f(hb.w)};
    float s = 0.f;
#pragma unroll
    for (int j = 0; j < 8; ++j) s += hv[j];
    s += __shfl_xor(s, 16); s += __shfl_xor(s, 32);
    const float mean = s * (1.f / 32.f);
    float m2v = 0.f;
#pragma unroll
    for (int j = 0; j < 8; ++j) { float d = hv[j] - mean; m2v += d * d; }
    m2v += __shfl_xor(m2v, 16); m2v += __shfl_xor(m2v, 32);
    if (g == 0) st8_sc1(rs, WS_HST + (unsigned)(((tilef * 16 + 2 * ct + khf) * 16 + r) * 8), (u32x2){__float_as_uint(mean), __float_as_uint(m2v)});
  }
  asm volatile("" : "+v"(hb.x), "+v"(hb.y), "+v"(hb.z), "+v"(hb.w), "+v"(xo_off));
  TS(34);
  WAIT_VM(0);
  TS(35);
  bar();
  if (tid == 0) {
    st8_sc1(rs, WS_F3 + (unsigned)((P * 16 + C) * 8), (u32x2){tlo, thi});
    if (bid == 0) st8_sc1(rs, WS_E, (u32x2){tlo, thi});
  }
  TS(8);
  const int m3 = w >> 1, kh3 = w & 1;
  unsigned rd7[7];
  {
    const int rho0 = 4 * g + q;
#pragma unroll
    for (int t = 0; t < 4; ++t) rd7[t] = rd4[t];
#pragma unroll
    for (int b = 0; b < 2; ++b) rd7[4 + b] = 4096u + (unsigned)(64 * rho0 + 16 * (pp ^ ((rho0 >> 2) & 1)) + 8 * (b ^ (pp & 1)));
    rd7[6] = 6144u + (unsigned)(32 * rho0 + 8 * pp);
  }
  f32x4 cs[7];
  {
    if (w < 7) {
      const unsigned a0 = w == 0 ? rd7[0] : w == 1 ? rd7[1] : w == 2 ? rd7[2] : w == 3 ? rd7[3] : w == 4 ? rd7[4] : w == 5 ? rd7[5] : rd7[6];
      const unsigned a1 = a0 + (w < 4 ? 2048u : w < 6 ? 1024u : 512u);
      f32x4 c0 = zero4, c1 = zero4;
#pragma unroll
      for (int i = 0; i < 16; i += 2) {
        c0 = mfma32(tr_frag(lds, a0 + i * 7168, a1 + i * 7168), ones, c0);
        c1 = mfma32(tr_frag(lds, a0 + (i + 1) * 7168, a1 + (i + 1) * 7168), ones, c1);
      }
      if (r == 0) *(LAS f32x4*)(lds + L_SP + w * 64 + g * 16) = c0 + c1;
    }
    bar();
#pragma unroll
    for (int t = 0; t < 7; ++t) cs[t] = *(LAS f32x4*)(lds + L_SP + t * 64 + g * 16);
    bar();
    TS(36);
  }
  const unsigned w2col = (unsigned)(ct * 128);
  auto issue_w2 = [&](int kqq, int il, int b) {
    const int rho = b * 8 + (lane >> 3);
    glds16(w2, (unsigned)(w2_row(kqq, il, rho) * 1024) + w2col + (unsigned)(((lane & 7) ^ ((rho >> 1) & 3)) * 16), ldsb + w2_slot(kqq, il) * 4096 + b * 1024);
  };
#pragma unroll
  for (int il = 0; il < 3; ++il) { issue_w2(kq, il, 2 * m2); issue_w2(kq, il, 2 * m2 + 1); }
  const int tile3 = P * 8 + 2 * m3;
  const int tilef3 = tile3 + kh3;
  wait_flags(ws, WS_F3 + (unsigned)((P * 16 + (m3 >> 1) * 8 + (lane & 7)) * 8), tlo, thi);
  TS(9);
  {
    u32x4 hf[2][8];
#pragma unroll
    for (int i = 0; i < 8; ++i)
#pragma unroll
      for (int tt = 0; tt < 2; ++tt) hf[tt][i] = ld16_sc1(rs, (unsigned)(lane * 16 + (i & 3) * 1024), WS_H + (unsigned)(((tile3 + tt) * 16 + kh3 * 8 + (i & 4)) * 1024));
    u32x2 st[4];
#pragma unroll
    for (int i = 0; i < 4; ++i) st[i] = ld8_sc1(rs, WS_HST + (unsigned)(((tilef3 * 16 + 4 * g + i) * 16 + r) * 8));
    f32x4 acc[2][7];
#pragma unroll
    for (int t = 0; t < 7; ++t) acc[0][t] = acc[1][t] = zero4;
    s16x8 f[2][7];
    u32x4 bA, bB, bC; u32x2 bD;
    const unsigned kbase = (unsigned)(kh3 * 8 * 7168);
#pragma unroll
    for (int i = 0; i <= 8; ++i) {
      if (i < 8) {
#pragma unroll
        for (int t = 0; t < 7; ++t) { const unsigned a = kbase + i * 7168 + rd7[t]; f[i & 1][t] = tr_frag(lds, a, a + (t < 4 ? 2048 : t < 6 ? 1024 : 512)); }
        SCHED_FENCE();
      }
      if (i > 0) {
        const int i0 = i - 1;
#pragma unroll
        for (int t = 0; t < 7; ++t) {
          acc[0][t] = mfma32(f[i0 & 1][t], as_frag(hf[0][i0]), acc[0][t]);
          acc[1][t] = mfma32(f[i0 & 1][t], as_frag(hf[1][i0]), acc[1][t]);
        }
        SCHED_FENCE();
      }
      if (i == 4) {
#pragma unroll
        for (int k = 4; k < 8; ++k) asm volatile("" : "+v"(hf[0][k]), "+v"(hf[1][k]));
        asm volatile("" : "+v"(st[0]), "+v"(st[1]), "+v"(st[2]), "+v"(st[3]));
        asm volatile("" ::: "memory");
        TS(37);
        __builtin_amdgcn_s_barrier();
        asm volatile("" ::: "memory");
        {
          const unsigned bo = (unsigned)((C * 112 + 16 * g) * 2), bo2 = (unsigned)((C * 112 + 64 + 8 * g) * 2), bo3 = (unsigned)((C * 112 + 96 + 4 * g) * 2);
          asm volatile("global_load_dwordx4 %0, %1, %2" : "=v"(bA) : "v"(bo), "s"(b1) : "memory");
          asm volatile("global_load_dwordx4 %0, %1, %2 offset:16" : "=v"(bB) : "v"(bo), "s"(b1) : "memory");
          asm volatile("global_load_dwordx4 %0, %1, %2" : "=v"(bC) : "v"(bo2), "s"(b1) : "memory");
          asm volatile("global_load_dwordx2 %0, %1, %2" : "=v"(bD) : "v"(bo3), "s"(b1) : "memory");
        }
#pragma unroll
        for (int il = 3; il < 6; ++il) { issue_w2(kq, il, 2 * m2); issue_w2(kq, il, 2 * m2 + 1); }
        issue_w2(w >> 2, 6, w & 3);
        SCHED_FENCE();
      }
    }
    TS(38);
    WAIT_VM(7);
    asm volatile("" : "+v"(bA), "+v"(bB), "+v"(bC), "+v"(bD));
    bar();
    TS(10);
    const unsigned ex_own = (unsigned)((w < 4 ? 28672 : 86016 - 4 * 7168) + w * 7168), ex_oth = (unsigned)((w < 4 ? 28672 : 86016 - 4 * 7168) + (w ^ 1) * 7168);
#pragma unroll
    for (int t = 0; t < 7; ++t) *(LAS f32x4*)(lds + ex_own + t * 1024 + lane * 16) = kh3 ? acc[0][t] : acc[1][t];
    float ms = (__uint_as_float(st[0].x) + __uint_as_float(st[1].x)) + (__uint_as_float(st[2].x) + __uint_as_float(st[3].x));
    ms += __shfl_xor(ms, 16); ms += __shfl_xor(ms, 32);
    const float mean = ms * (1.f / 16.f);
    float m2v = 0.f;
#pragma unroll
    for (int i = 0; i < 4; ++i) { float d = __uint_as_float(st[i].x) - mean; m2v += __uint_as_float(st[i].y) + 32.f * d * d; }
    m2v += __shfl_xor(m2v, 16); m2v += __shfl_xor(m2v, 32);
    const float var = m2v * (1.f / 512.f);
    const float r1 = rsqrtf(var + 1e-5f);
    const float rr = r1 * rsqrtf(var * r1 * r1 + 1e-5f);
    bar();
    TS(39);
    f32x4 v[7];
#pragma unroll
    for (int t = 0; t < 7; ++t) {
      v[t] = (kh3 ? acc[1][t] : acc[0][t]) + *(LAS f32x4*)(lds + ex_oth + t * 1024 + lane * 16);
      v[t] = (v[t] - mean * cs[t]) * rr;
    }
    const unsigned dst = WS_HH + (unsigned)((tilef3 * 16 + C) * 3584);
    const bool od = g & 1;
#pragma unroll
    for (int u = 0; u < 3; ++u) {
      const f32x4 A = od ? v[2 * u + 1] : v[2 * u], B = od ? v[2 * u] : v[2 * u + 1];
      const u32x4 bb = u == 0 ? bA : u == 1 ? bB : bC;
      u32x4 o;
      o.x = pk_bf16(gelu_tanh(A[0] + lo_f(bb.x)), gelu_tanh(A[1] + hi_f(bb.x)));
      o.y = pk_bf16(gelu_tanh(A[2] + lo_f(bb.y)), gelu_tanh(A[3] + hi_f(bb.y)));
      o.z = pk_bf16(gelu_tanh(B[0] + lo_f(bb.z)), gelu_tanh(B[1] + hi_f(bb.z)));
      o.w = pk_bf16(gelu_tanh(B[2] + lo_f(bb.w)), gelu_tanh(B[3] + hi_f(bb.w)));
      st16_sc1(rs, dst + u * 1024 + lane * 16, o);
    }
    {
      u32x2 o;
      o.x = pk_bf16(gelu_tanh(v[6][0] + lo_f(bD.x)), gelu_tanh(v[6][1] + hi_f(bD.x)));
      o.y = pk_bf16(gelu_tanh(v[6][2] + lo_f(bD.y)), gelu_tanh(v[6][3] + hi_f(bD.y)));
      st8_sc1(rs, dst + 3072 + lane * 8, o);
    }
  }
  TS(40);
  WAIT_VM(0);
  TS(41);
  bar();
  if (tid == 0) st8_sc1(rs, WS_F4 + (unsigned)((P * 16 + C) * 8), (u32x2){tlo, thi});
  TS(11);
  {
#pragma unroll
    for (int il = 11; il < 14; ++il) { issue_w2(kq, il, 2 * m2); issue_w2(kq, il, 2 * m2 + 1); }
    issue_w2(2 + (w >> 2), 6, w & 3);
    s16x8 wr[4][4];
#pragma unroll
    for (int il = 0; il < 4; ++il)
#pragma unroll
      for (int t = 0; t < 4; ++t) { const unsigned a = (unsigned)(w2_slot(kq, il) * 4096) + rd4[t]; wr[il][t] = tr_frag(lds, a, a + 2048); }
    bar();
    TS(42);
#pragma unroll
    for (int il = 7; il < 11; ++il) { issue_w2(kq, il, 2 * m2); issue_w2(kq, il, 2 * m2 + 1); }
    const u32x4 bb = *(const u32x4*)(b2 + ct * 64 + 16 * g + 8 * khf);
    wait_flags(ws, WS_F4 + (unsigned)((P * 16 + 4 * kq + (lane & 3)) * 8), tlo, thi);
    TS(12);
    u32x4 af[2][14];
#pragma unroll
    for (int i = 0; i < 14; ++i)
#pragma unroll
      for (int tt = 0; tt < 2; ++tt) {
        const unsigned src = WS_HH + (unsigned)(((tile0 + tt) * 16 + 4 * kq) * 3584);
        if (i < 12) af[tt][i] = ld16_sc1(rs, (unsigned)((i % 3) * 1024 + lane * 16), src + (i / 3) * 3584);
        else {
          u32x2 a = ld8_sc1(rs, (unsigned)(3072 + lane * 8), src + (2 * (i - 12)) * 3584), b = ld8_sc1(rs, (unsigned)(3072 + lane * 8), src + (2 * (i - 12) + 1) * 3584);
          af[tt][i] = (u32x4){a.x, a.y, b.x, b.y};
        }
      }
    bar();
    TS(43);
    f32x4 acc[2][4];
#pragma unroll
    for (int t = 0; t < 4; ++t) acc[0][t] = acc[1][t] = zero4;
    s16x8 f[2][4];
#pragma unroll
    for (int i = 0; i < 14; ++i) {
      if (i + 1 >= 4 && i + 1 < 14) {
#pragma unroll
        for (int t = 0; t < 4; ++t) { const unsigned a = (unsigned)(w2_slot(kq, i + 1) * 4096) + rd4[t]; f[(i + 1) & 1][t] = tr_frag(lds, a, a + 2048); }
        SCHED_FENCE();
      }
#pragma unroll
      for (int t = 0; t < 4; ++t) {
        const s16x8 wa = i < 4 ? wr[i & 3][t] : f[i & 1][t];
        acc[0][t] = mfma32(wa, as_frag(af[0][i]), acc[0][t]);
        acc[1][t] = mfma32(wa, as_frag(af[1][i]), acc[1][t]);
      }
      SCHED_FENCE();
    }
    TS(44);
    bar();
    TS(13);
    f32x4 k0 = zero4, k1 = zero4;
#pragma unroll
    for (int d = 0; d < 4; ++d) {
      if (d == kq) { k0 = acc[d >> 1][2 * (d & 1)]; k1 = acc[d >> 1][2 * (d & 1) + 1]; }
      else {
        LAS char* dst = lds + ((m2 * 4 + d) * 3 + ((kq - d - 1) & 3)) * 2048 + lane * 16;
        *(LAS f32x4*)dst = acc[d >> 1][2 * (d & 1)];
        *(LAS f32x4*)(dst + 1024) = acc[d >> 1][2 * (d & 1) + 1];
      }
    }
    bar();
#pragma unroll
    for (int s = 0; s < 3; ++s) {
      k0 += *(LAS f32x4*)(lds + (w * 3 + s) * 2048 + lane * 16);
      k1 += *(LAS f32x4*)(lds + (w * 3 + s) * 2048 + 1024 + lane * 16);
    }
    const f32x4 A = (g & 1) ? k1 : k0, B = (g & 1) ? k0 : k1;
    u32x4 o;
    o.x = pk_bf16(lo_f(hb.x) + A[0] + lo_f(bb.x), hi_f(hb.x) + A[1] + hi_f(bb.x));
    o.y = pk_bf16(lo_f(hb.y) + A[2] + lo_f(bb.y), hi_f(hb.y) + A[3] + hi_f(bb.y));
    o.z = pk_bf16(lo_f(hb.z) + B[0] + lo_f(bb.z), hi_f(hb.z) + B[1] + hi_f(bb.z));
    o.w = pk_bf16(lo_f(hb.w) + B[2] + lo_f(bb.w), hi_f(hb.w) + B[3] + hi_f(bb.w));
    *(u32x4*)((char*)out + xo_off) = o;
  }
  TS(14);
}

extern "C" void kernel_launch(void* const* d_in, const int* in_sizes, int n_in, void* d_out, int out_size, void* d_ws, size_t ws_size, hipStream_t stream) {
  const bf16_t* x = (const bf16_t*)d_in[0]; const bf16_t* wqkv = (const bf16_t*)d_in[1]; const bf16_t* wo = (const bf16_t*)d_in[2];
  const bf16_t* w1 = (const bf16_t*)d_in[4]; const bf16_t* b1 = (const bf16_t*)d_in[5]; const bf16_t* w2 = (const bf16_t*)d_in[6]; const bf16_t* b2 = (const bf16_t*)d_in[7];
  (void)hipFuncSetAttribute((const void*)fused_block, hipFuncAttributeMaxDynamicSharedMemorySize, 163840);
  fused_block<<<256, 512, 163840, stream>>>(x, wqkv, wo, w1, b1, w2, b2, (char*)d_ws, (bf16_t*)d_out);
}
```

```cpp
#include <hip/hip_runtime.h>
#include <stdint.h>
#define WARM 7

typedef unsigned short bf16_t;
typedef unsigned long long u64;
typedef __attribute__((ext_vector_type(8))) short s16x8;
typedef __attribute__((ext_vector_type(4))) short s16x4;
typedef __attribute__((ext_vector_type(4))) float f32x4;
typedef __attribute__((ext_vector_type(2))) float f32x2;
typedef __attribute__((ext_vector_type(4))) unsigned u32x4;
typedef __attribute__((ext_vector_type(2))) unsigned u32x2;
#define LAS __attribute__((address_space(3)))
#define DI __device__ __forceinline__

constexpr unsigned WS_E    = 0;
constexpr unsigned WS_F1   = 4096;
constexpr unsigned WS_F2   = 4096 + 2048;
constexpr unsigned WS_F3   = 4096 + 4096;
constexpr unsigned WS_F4   = 4096 + 6144;
constexpr unsigned WS_KVP  = 65536;
constexpr unsigned WS_HST  = 655360;
constexpr unsigned WS_O    = 1u << 20;
constexpr unsigned WS_H    = 3u << 20;
constexpr unsigned WS_HH   = 5u << 20;
constexpr int KVP_STRIDE = 2176;

DI unsigned pk_bf16(float lo, float hi) { unsigned r; asm("v_cvt_pk_bf16_f32 %0, %1, %2" : "=v"(r) : "v"(lo), "v"(hi)); return r; }
DI float lo_f(unsigned u) { return __uint_as_float(u << 16); }
DI float hi_f(unsigned u) { return __uint_as_float(u & 0xffff0000u); }

DI void glds16(const void* sbase, unsigned voff, unsigned lds_dst) {
  unsigned keep;
  asm volatile("s_mov_b32 %0, m0\n\ts_mov_b32 m0, %3\n\ts_nop 0\n\tglobal_load_lds_dwordx4 %1, %2\n\ts_mov_b32 m0, %0"
               : "=&s"(keep) : "v"(voff), "s"(sbase), "s"(lds_dst) : "memory");
}
#define WAIT_VM(n) asm volatile("s_waitcnt vmcnt(" #n ")" ::: "memory")
DI void bar() { asm volatile("s_waitcnt lgkmcnt(0)" ::: "memory"); __builtin_amdgcn_s_barrier(); asm volatile("" ::: "memory"); }
#define SCHED_FENCE() __builtin_amdgcn_sched_barrier(0)

DI s16x8 tr_frag(LAS char* lds, unsigned a0, unsigned a1) {
  s16x4 a = __builtin_amdgcn_ds_read_tr16_b64_v4i16((LAS s16x4*)(lds + a0));
  s16x4 b = __builtin_amdgcn_ds_read_tr16_b64_v4i16((LAS s16x4*)(lds + a1));
  return __builtin_shufflevector(a, b, 0, 1, 2, 3, 4, 5, 6, 7);
}
DI f32x4 mfma32(s16x8 a, s16x8 b, f32x4 c) { return __builtin_amdgcn_mfma_f32_16x16x32_bf16(a, b, c, 0, 0, 0); }
DI f32x4 mfma16(s16x4 a, s16x4 b, f32x4 c) { return __builtin_amdgcn_mfma_f32_16x16x16bf16_1k(a, b, c, 0, 0, 0); }
DI float phi(float t) { return (t > 0.f ? t + 1.f : __expf(t)) + 1e-6f; }
DI s16x8 as_frag(u32x4 v) { return __builtin_bit_cast(s16x8, v); }

typedef __amdgpu_buffer_rsrc_t rsrc_t;
#define XAUX 16
DI u32x4 ld16_sc1(rsrc_t rs, unsigned off) { return __builtin_amdgcn_raw_buffer_load_b128(rs, off, 0, 16); }
DI u32x4 ld16_sc1(rsrc_t rs, unsigned off, unsigned soff) { return __builtin_amdgcn_raw_buffer_load_b128(rs, off, soff, XAUX); }
DI u32x2 ld8_sc1(rsrc_t rs, unsigned off, unsigned soff = 0) { return __builtin_amdgcn_raw_buffer_load_b64(rs, off, soff, 16); }
DI void st16_sc1(rsrc_t rs, unsigned off, u32x4 v) { __builtin_amdgcn_raw_buffer_store_b128(v, rs, off, 0, 16); }
DI void st8_sc1(rsrc_t rs, unsigned off, u32x2 v) { __builtin_amdgcn_raw_buffer_store_b64(v, rs, off, 0, 16); }

DI void wait_flags(const char* ws, unsigned off, unsigned tlo, unsigned thi) {
  for (int it = 0; it < (1 << 18); ++it) {
    u32x2 v;
    asm volatile("global_load_dwordx2 %0, %1, %2 sc1\n\ts_waitcnt vmcnt(0)" : "=v"(v) : "v"(off), "s"(ws) : "memory");
    const bool ok = (v.x == tlo) & (v.y == thi);
    if (__builtin_amdgcn_ballot_w64(ok) == ~0ull) break;
    __builtin_amdgcn_s_sleep(1);
  }
  asm volatile("" ::: "memory");
}

#define TS(i)

DI unsigned g64_rd(int g, int q, int pp, int u, int b) {
  int rho = 4 * g + q;
  return (unsigned)(128 * rho + 16 * ((2 * pp + u) ^ ((rho >> 1) & 3)) + 8 * (b ^ (pp & 1)));
}
DI float gelu_tanh(float v) {
  float t = v * v;
  float a = v * (-2.3022082f - 0.1029432397f * t);
  float e = __builtin_amdgcn_exp2f(a);
  return v * __builtin_amdgcn_rcpf(1.f + e);
}
DI int w2_row(int kq, int i, int rho) {
  const int gg = (rho >> 2) & 3, j = 4 * (rho >> 4) + (rho & 3);
  if (i < 12) { int cp = 4 * kq + i / 3, u = i % 3; return u < 2 ? 112 * cp + 16 * gg + 8 * u + j : 112 * cp + 64 + 8 * gg + j; }
  int cp = 4 * kq + 2 * (i - 12) + (j >> 2);
  return 112 * cp + 96 + 4 * gg + (j & 3);
}
DI int slotB(int j) { return j < 7 ? j : j + 7; }
DI int slotC(int j) { return j < 7 ? 7 + j : 14 + j; }
DI int w2_slot(int kq, int il) {
  switch (il) {
    case 0: case 7: return 28 + kq;
    case 1: case 8: return 32 + kq;
    case 2: case 9: return 36 + kq;
    case 3: case 10: return slotB(kq);
    case 4: return slotB(4 + kq);
    case 5: return slotB(8 + kq);
    case 6: return kq < 2 ? slotB(12 + kq) : slotC(10 + kq);
    case 11: return slotC(kq);
    case 12: return slotC(4 + kq);
    default: return slotC(8 + kq);
  }
}

__global__ __launch_bounds__(512, 2) void fused_block(const bf16_t* __restrict__ x, const bf16_t* __restrict__ wqkv, const bf16_t* __restrict__ wo,
                                                      const bf16_t* __restrict__ w1, const bf16_t* __restrict__ b1, const bf16_t* __restrict__ w2,
                                                      const bf16_t* __restrict__ b2, char* ws, bf16_t* __restrict__ out) {
  extern __shared__ __attribute__((aligned(16))) char smem_[];
  LAS char* lds = (LAS char*)smem_;
  const int tid = threadIdx.x, lane = tid & 63;
  const int w = __builtin_amdgcn_readfirstlane(tid >> 6);
  const int bid = blockIdx.x;
  const int P = (bid & 7) * 2 + (bid >> 7), C = (bid >> 3) & 15;
  const int g = lane >> 4, r = lane & 15, q = (lane >> 2) & 3, pp = lane & 3;
  const unsigned ldsb = (unsigned)(size_t)smem_;
  const rsrc_t rs = __builtin_amdgcn_make_buffer_rsrc(ws, 0, 0x7fffffff, 0x00020000);

  {
    const u64 t0 = __builtin_amdgcn_s_memrealtime();
    unsigned accd = 0;
    const unsigned li = (unsigned)(((bid >> 3) * 512 + tid) * 16);
    auto touch = [&](const void* p, unsigned bytes) { for (unsigned o = li; o < bytes; o += 262144) { u32x4 v = *(const u32x4*)((const char*)p + o); accd ^= v.x ^ v.y ^ v.z ^ v.w; } };
    touch(ws, 12u << 20); touch(out, 2097152);
    touch(w2, 1835008); touch(w1, 1835008); touch(wo, 524288); touch(b1, 3584); touch(b2, 1024); touch(x, 2097152); touch(wqkv, 1572864);
    { u64 pc; asm volatile("s_getpc_b64 %0" : "=s"(pc)); if (li < 30720) { u32x4 v = *(const u32x4*)((const char*)pc + li); accd ^= v.x ^ v.y ^ v.z ^ v.w; } }
    asm volatile("" :: "v"(accd));
    for (int it = 0; it < 100000; ++it) { if (__builtin_amdgcn_s_memrealtime() - t0 >= 7000) break; __builtin_amdgcn_s_sleep(2); }
  }
  TS(0);
  u32x2 Ev;
  asm volatile("global_load_dwordx2 %0, %1, %2 sc1" : "=v"(Ev) : "v"(WS_E), "s"(ws) : "memory");

  s16x8 ones; for (int j = 0; j < 8; ++j) ones[j] = (short)0x3f80;
  const f32x4 zero4 = (f32x4){0, 0, 0, 0};
  const int ct = C & 7, th = C >> 3;

  constexpr unsigned L_WKV = 0, L_WQ = 65536, L_X = 98304;
  const int m1 = w >> 1, n1 = w & 1;
  u32x4 pq;
  unsigned tlo, thi;
  {
    const int xrow = lane >> 3;
    const unsigned xoff = (unsigned)(((P * 128 + w * 16 + xrow) * 512) * 2 + (((lane & 7) ^ (xrow & 7)) * 16));
    const int wk = lane >> 2;
    const unsigned wcol = (unsigned)((((((lane >> 1) & 1) ^ ((wk >> 3) & 1)) * 16) + (lane & 1) * 8) * 2);
    const unsigned woff = (unsigned)(wk * 1536 * 2) + wcol + (unsigned)(C * 64);
    auto issue_x = [&](int ch) {
      unsigned dst = ldsb + L_X + (ch & 3) * 16384 + w * 2048;
      glds16(x, xoff + ch * 128, dst);
      glds16(x, xoff + ch * 128 + 8 * 1024, dst + 1024);
    };
    auto issue_wkv = [&](int ch) {
      int sub = w >> 1, seg = w & 1;
      glds16(wqkv, woff + (unsigned)((ch * 64 + sub * 16) * 3072 + (1 + seg) * 1024), ldsb + L_WKV + ch * 8192 + w * 1024);
    };
    auto issue_wq = [&](int i) {
      int blk = w * 4 + i;
      glds16(wqkv, woff + (unsigned)(blk * 16 * 3072), ldsb + L_WQ + blk * 1024);
    };
#pragma unroll
    for (int ch = 0; ch < 4; ++ch) { issue_x(ch); issue_wkv(ch); }
#pragma unroll
    for (int ch = 4; ch < 8; ++ch) issue_wkv(ch);

    TS(53);
    const unsigned xrd_own = L_X + w * 2048 + r * 128, xrd_oth = L_X + (w ^ 1) * 2048 + r * 128;
    const unsigned xsw0 = (unsigned)(((0 + g) ^ (r & 7)) * 16), xsw1 = (unsigned)(((4 + g) ^ (r & 7)) * 16);
    const unsigned wrd = (unsigned)((g >> 1) * 1024 + (8 * (g & 1) + q) * 64 + 8 * pp);
    const unsigned wrd_n = L_WKV + (g >> 1) * 1024 + wrd + 32u * (n1 ^ (g & 1));

    s16x8 xf[16];
    s16x8 xo[2][2], wf[2][2][2];
    f32x4 acc[2][2];
    f32x4 cs[2], sx = zero4, gx = zero4;
    acc[0][0] = acc[0][1] = acc[1][0] = acc[1][1] = cs[0] = cs[1] = zero4;
#pragma unroll
    for (int ch = 0; ch <= 8; ++ch) {
      if (ch < 8) {
        if (ch == 0) WAIT_VM(13); else if (ch == 1) WAIT_VM(10); else if (ch == 2) WAIT_VM(9); else if (ch == 3) WAIT_VM(8);
        else if (ch == 4) WAIT_VM(4); else if (ch == 5) WAIT_VM(8); else if (ch == 6) WAIT_VM(6); else WAIT_VM(4);
        bar();
        if (ch == 0) TS(45); if (ch == 1) TS(46); if (ch == 2) TS(47); if (ch == 3) TS(48); if (ch == 4) TS(49); if (ch == 5) TS(50); if (ch == 6) TS(51); if (ch == 7) TS(52);
        if (ch >= 1 && ch <= 4) issue_x(ch + 3);
        if (ch == 4) {
#pragma unroll
          for (int i = 0; i < 4; ++i) issue_wq(i);
        }
#pragma unroll
        for (int s = 0; s < 2; ++s) {
          xf[ch * 2 + s] = *(LAS s16x8*)(lds + xrd_own + (ch & 3) * 16384 + (s ? xsw1 : xsw0));
          xo[ch & 1][s] = *(LAS s16x8*)(lds + xrd_oth + (ch & 3) * 16384 + (s ? xsw1 : xsw0));
#pragma unroll
          for (int seg = 0; seg < 2; ++seg) {
            const unsigned a = wrd_n + ch * 8192 + s * 4096 + seg * 1024;
            wf[ch & 1][s][seg] = tr_frag(lds, a, a + 256);
          }
        }
        SCHED_FENCE();
      }
      if (ch > 0) {
        const int c0 = ch - 1;
#pragma unroll
        for (int s = 0; s < 2; ++s) {
          const s16x8 xa = xf[c0 * 2 + s], xb = xo[c0 & 1][s];
          acc[0][0] = mfma32(xa, wf[c0 & 1][s][0], acc[0][0]);
          acc[0][1] = mfma32(xa, wf[c0 & 1][s][1], acc[0][1]);
          acc[1][0] = mfma32(xb, wf[c0 & 1][s][0], acc[1][0]);
          acc[1][1] = mfma32(xb, wf[c0 & 1][s][1], acc[1][1]);
          sx = mfma32(xa, ones, sx);
          gx = mfma32(xa, xa, gx);
        }
        if ((c0 >> 1) == m1) {
#pragma unroll
          for (int s = 0; s < 2; ++s) {
            cs[0] = mfma32(ones, wf[c0 & 1][s][0], cs[0]);
            cs[1] = mfma32(ones, wf[c0 & 1][s][1], cs[1]);
          }
        }
        SCHED_FENCE();
      }
    }
    TS(1);
    {
      LAS float* sc = (LAS float*)(lds + w * 256);
      if (g == 0) { sc[r] = cs[0][0]; sc[16 + r] = cs[1][0]; }
      if (r == 0) *(LAS f32x4*)(sc + 32 + 4 * g) = sx;
      if ((r >> 2) == g) { float d = (r & 3) == 0 ? gx[0] : (r & 3) == 1 ? gx[1] : (r & 3) == 2 ? gx[2] : gx[3]; sc[48 + r] = d; }
    }
    bar();
    TS(20);
    if (w >= 4) {
      const int row = lane >> 3;
      const unsigned off = (unsigned)(row * 1024 + ct * 128 + (((lane & 7) ^ ((row >> 1) & 3)) * 16));
#pragma unroll
      for (int i = 0; i < 16; ++i) { int blk = (w - 4) * 16 + i; glds16(wo, off + blk * 8192, ldsb + L_X + blk * 1024); }
    }
    TS(21);
    float mean1, rstd1;
    {
      float ck = 0.f, cv = 0.f;
#pragma unroll
      for (int mm = 0; mm < 4; ++mm) { LAS float* sc = (LAS float*)(lds + (2 * mm + n1) * 256); ck += sc[r]; cv += sc[16 + r]; }
      float pk[8], vv[8];
#pragma unroll
      for (int t = 0; t < 2; ++t) {
        LAS float* sc = (LAS float*)(lds + (w ^ t) * 256);
        f32x4 S = *(LAS f32x4*)(sc + 32 + 4 * g), Q = *(LAS f32x4*)(sc + 48 + 4 * g);
#pragma unroll
        for (int j = 0; j < 4; ++j) {
          float mu = S[j] * (1.f / 512.f); float var = fmaxf(Q[j] * (1.f / 512.f) - mu * mu, 0.f); float rsd = rsqrtf(var + 1e-5f);
          pk[t * 4 + j] = phi(rsd * (acc[t][0][j] - mu * ck));
          vv[t * 4 + j] = rsd * (acc[t][1][j] - mu * cv);
        }
      }
      {
        LAS float* sc = (LAS float*)(lds + w * 256);
        float mu = sc[32 + r] * (1.f / 512.f); float var = fmaxf(sc[48 + r] * (1.f / 512.f) - mu * mu, 0.f); mean1 = mu; rstd1 = rsqrtf(var + 1e-5f);
      }
      u32x4 pk8, v8;
      pk8.x = pk_bf16(pk[0], pk[1]); pk8.y = pk_bf16(pk[2], pk[3]); pk8.z = pk_bf16(pk[4], pk[5]); pk8.w = pk_bf16(pk[6], pk[7]);
      v8.x = pk_bf16(vv[0], vv[1]); v8.y = pk_bf16(vv[2], vv[3]); v8.z = pk_bf16(vv[4], vv[5]); v8.w = pk_bf16(vv[6], vv[7]);
      f32x4 kvp = mfma32(as_frag(pk8), as_frag(v8), zero4);
      f32x4 zp = mfma32(as_frag(pk8), ones, zero4);
      LAS char* sc = lds + 4096 + w * 1088;
      *(LAS f32x4*)(sc + lane * 16) = kvp;
      if (r == 0) *(LAS f32x4*)(sc + 1024 + g * 16) = zp;
    }
    TS(22);
    if (w >= 4) WAIT_VM(16); else WAIT_VM(0);
    TS(23);
    bar();
    TS(24);
    asm volatile("" : "+v"(Ev));
    {
      const u64 E = ((u64)(unsigned)__builtin_amdgcn_readfirstlane(Ev.y) << 32) | (unsigned)__builtin_amdgcn_readfirstlane(Ev.x);
      const u64 T = E * 0x9E3779B97F4A7C15ull + 0xD1B54A32D192ED03ull;
      tlo = (unsigned)T; thi = (unsigned)(T >> 32);
    }
    if (w == 0) {
#pragma unroll
      for (int rd = 0; rd < 3; ++rd) {
        const int i = rd * 64 + lane;
        if (i < 136) {
          const int hd = i >= 68, wi = i - hd * 68;
          f32x4 s = zero4;
#pragma unroll
          for (int mm = 0; mm < 4; ++mm) s += *(LAS f32x4*)(lds + 4096 + (2 * mm + hd) * 1088 + wi * 16);
          st16_sc1(rs, WS_KVP + (unsigned)((C * 16 + P) * KVP_STRIDE + i * 16), __builtin_bit_cast(u32x4, s));
        }
      }
    }
    TS(2);
    f32x4 aq[2], csq[2];
    aq[0] = aq[1] = csq[0] = csq[1] = zero4;
    {
      const unsigned a0 = L_WQ + wrd + 32u * (0 ^ (g & 1)), a1 = L_WQ + wrd + 32u * (1 ^ (g & 1));
      s16x8 f[2][4];
#pragma unroll
      for (int kk = 0; kk <= 8; ++kk) {
        if (kk < 8) {
#pragma unroll
          for (int s = 0; s < 2; ++s) {
            f[kk & 1][s * 2 + 0] = tr_frag(lds, a0 + (kk * 2 + s) * 2048, a0 + (kk * 2 + s) * 2048 + 256);
            f[kk & 1][s * 2 + 1] = tr_frag(lds, a1 + (kk * 2 + s) * 2048, a1 + (kk * 2 + s) * 2048 + 256);
          }
          SCHED_FENCE();
        }
        if (kk > 0) {
          const int k0 = kk - 1;
#pragma unroll
          for (int s = 0; s < 2; ++s) {
            aq[0] = mfma32(f[k0 & 1][s * 2 + 0], xf[k0 * 2 + s], aq[0]);
            aq[1] = mfma32(f[k0 & 1][s * 2 + 1], xf[k0 * 2 + s], aq[1]);
            csq[0] = mfma32(f[k0 & 1][s * 2 + 0], ones, csq[0]);
            csq[1] = mfma32(f[k0 & 1][s * 2 + 1], ones, csq[1]);
          }
          SCHED_FENCE();
        }
      }
    }
    TS(25);
    if (w == 0) {
      WAIT_VM(0);
      if (lane == 0) st8_sc1(rs, WS_F1 + (unsigned)((C * 16 + P) * 8), (u32x2){tlo, thi});
    }
    {
      float a[4], b[4];
#pragma unroll
      for (int j = 0; j < 4; ++j) { a[j] = phi(rstd1 * (aq[0][j] - mean1 * csq[0][j])); b[j] = phi(rstd1 * (aq[1][j] - mean1 * csq[1][j])); }
      pq.x = pk_bf16(a[0], a[1]); pq.y = pk_bf16(a[2], a[3]); pq.z = pk_bf16(b[0], b[1]); pq.w = pk_bf16(b[2], b[3]);
    }
  }
  bar();
  TS(3);
  auto issue_w1 = [&](int kb) {
    auto srow = [&](int rho) { return 64 * (kb >> 1) + 16 * ((rho >> 2) & 3) + 8 * (kb & 1) + 4 * (rho >> 4) + (rho & 3); };
    const unsigned dst = ldsb + kb * 7168;
#pragma unroll
    for (int b = 0; b < 4; ++b) { int rho = b * 8 + (lane >> 3); glds16(w1, (unsigned)(srow(rho) * 3584 + C * 224 + (((lane & 7) ^ ((rho >> 1) & 3)) * 16)), dst + b * 1024); }
#pragma unroll
    for (int b = 0; b < 2; ++b) { int rho = b * 16 + (lane >> 2); glds16(w1, (unsigned)(srow(rho) * 3584 + C * 224 + 128 + (((lane & 3) ^ ((rho >> 2) & 1)) * 16)), dst + 4096 + b * 1024); }
    { int rho = lane >> 1; glds16(w1, (unsigned)(srow(rho) * 3584 + C * 224 + 192 + (lane & 1) * 16), dst + 6144); }
  };
  issue_w1(w);
  const int m2 = w >> 2, kq = w & 3;
  unsigned rd4[4];
#pragma unroll
  for (int t = 0; t < 4; ++t) rd4[t] = g64_rd(g, q, pp, t >> 1, t & 1);
  TS(26);
  WAIT_VM(7);
  TS(27);
  bar();
  TS(28);
  s16x8 wof[4][4];
#pragma unroll
  for (int i = 0; i < 4; ++i)
#pragma unroll
    for (int t = 0; t < 4; ++t) { const unsigned a = L_X + (unsigned)((kq * 4 + i) * 4096) + rd4[t]; wof[i][t] = tr_frag(lds, a, a + 2048); }
  bar();
  TS(4);
  constexpr unsigned L_SP = 114688;
  wait_flags(ws, WS_F1 + (unsigned)((C * 16 + 2 * w + (lane & 1)) * 8), tlo, thi);
  TS(5);
  {
    const unsigned base = WS_KVP + (unsigned)((C * 16 + 2 * w) * KVP_STRIDE);
    u32x4 a[3], b[3];
#pragma unroll
    for (int rd = 0; rd < 3; ++rd) {
      const int i = rd < 2 ? rd * 64 + lane : 128 + (lane & 7);
      a[rd] = ld16_sc1(rs, base + i * 16);
      b[rd] = ld16_sc1(rs, base + KVP_STRIDE + i * 16);
    }
#pragma unroll
    for (int rd = 0; rd < 3; ++rd) {
      const int i = rd < 2 ? rd * 64 + lane : 128 + (lane & 7);
      *(LAS f32x4*)(lds + L_SP + w * 2176 + i * 16) = __builtin_bit_cast(f32x4, a[rd]) + __builtin_bit_cast(f32x4, b[rd]);
    }
  }
  TS(29);
  bar();
  if (tid < 136) {
    f32x4 s = zero4;
#pragma unroll
    for (int ww = 0; ww < 8; ++ww) s += *(LAS f32x4*)(lds + L_SP + ww * 2176 + tid * 16);
    *(LAS u32x2*)(lds + L_SP + 17408 + tid * 8) = (u32x2){pk_bf16(s[0], s[1]), pk_bf16(s[2], s[3])};
  }
  bar();
  {
    u32x4 o;
#pragma unroll
    for (int hd = 0; hd < 2; ++hd) {
      s16x4 kvA = *(LAS s16x4*)(lds + L_SP + 17408 + (hd * 68 + lane) * 8);
      s16x4 zA = *(LAS s16x4*)(lds + L_SP + 17408 + (hd * 68 + 64 + g) * 8);
      u32x2 pu; pu.x = hd ? pq.z : pq.x; pu.y = hd ? pq.w : pq.y;
      s16x4 p4 = __builtin_bit_cast(s16x4, pu);
      f32x4 num = mfma16(kvA, p4, zero4);
      f32x4 den = mfma16(zA, p4, zero4);
      const float inv = 1.f / (den[0] + 1e-6f);
      if (hd == 0) { o.x = pk_bf16(num[0] * inv, num[1] * inv); o.y = pk_bf16(num[2] * inv, num[3] * inv); }
      else { o.z = pk_bf16(num[0] * inv, num[1] * inv); o.w = pk_bf16(num[2] * inv, num[3] * inv); }
    }
    st16_sc1(rs, WS_O + (unsigned)((((P * 8 + w) * 16 + C) * 64 + lane) * 16), o);
  }
  TS(30);
  WAIT_VM(0);
  TS(31);
  bar();
  if (tid == 0) st8_sc1(rs, WS_F2 + (unsigned)((P * 16 + C) * 8), (u32x2){tlo, thi});
  TS(6);
  issue_w1(8 + w);
  const int tile0 = P * 8 + th * 4 + 2 * m2;
  const int tilef = tile0 + (kq >> 1), khf = kq & 1;
  unsigned xo_off = (unsigned)(((tilef * 16 + r) * 512 + ct * 64 + 16 * g + 8 * khf) * 2);
  const u32x4 xr = *(const u32x4*)((const char*)x + xo_off);
  wait_flags(ws, WS_F2 + (unsigned)((P * 16 + 4 * kq + (lane & 3)) * 8), tlo, thi);
  TS(7);
  u32x4 hb;
  {
    u32x4 of[2][4];
#pragma unroll
    for (int i = 0; i < 4; ++i)
#pragma unroll
      for (int tt = 0; tt < 2; ++tt) of[tt][i] = ld16_sc1(rs, (unsigned)(lane * 16 + i * 1024), WS_O + (unsigned)(((tile0 + tt) * 16 + kq * 4) * 1024));
    SCHED_FENCE();
    f32x4 acc[2][4];
#pragma unroll
    for (int t = 0; t < 4; ++t) acc[0][t] = acc[1][t] = zero4;
#pragma unroll
    for (int i = 0; i < 4; ++i)
#pragma unroll
      for (int tt = 0; tt < 2; ++tt)
#pragma unroll
        for (int t = 0; t < 4; ++t) acc[tt][t] = mfma32(wof[i][t], as_frag(of[tt][i]), acc[tt][t]);
    f32x4 k0 = zero4, k1 = zero4;
#pragma unroll
    for (int d = 0; d < 4; ++d) {
      if (d == kq) { k0 = acc[d >> 1][2 * (d & 1)]; k1 = acc[d >> 1][2 * (d & 1) + 1]; }
      else {
        LAS char* dst = lds + L_SP + ((m2 * 4 + d) * 3 + ((kq - d - 1) & 3)) * 2048 + lane * 16;
        *(LAS f32x4*)dst = acc[d >> 1][2 * (d & 1)];
        *(LAS f32x4*)(dst + 1024) = acc[d >> 1][2 * (d & 1) + 1];
      }
    }
    TS(32);
    bar();
    TS(33);
#pragma unroll
    for (int s = 0; s < 3; ++s) {
      k0 += *(LAS f32x4*)(lds + L_SP + (w * 3 + s) * 2048 + lane * 16);
      k1 += *(LAS f32x4*)(lds + L_SP + (w * 3 + s) * 2048 + 1024 + lane * 16);
    }
    const f32x4 A = (g & 1) ? k1 : k0, B = (g & 1) ? k0 : k1;
    hb.x = pk_bf16(lo_f(xr.x) + A[0], hi_f(xr.x) + A[1]); hb.y = pk_bf16(lo_f(xr.y) + A[2], hi_f(xr.y) + A[3]);
    hb.z = pk_bf16(lo_f(xr.z) + B[0], hi_f(xr.z) + B[1]); hb.w = pk_bf16(lo_f(xr.w) + B[2], hi_f(xr.w) + B[3]);
    st16_sc1(rs, WS_H + (unsigned)(((tilef * 16 + 2 * ct + khf) * 64 + lane) * 16), hb);
    float hv[8] = {lo_f(hb.x), hi_f(hb.x), lo_f(hb.y), hi_f(hb.y), lo_f(hb.z), hi_f(hb.z), lo_f(hb.w), hi_f(hb.w)};
    float s = 0.f;
#pragma unroll
    for (int j = 0; j < 8; ++j) s += hv[j];
    s += __shfl_xor(s, 16); s += __shfl_xor(s, 32);
    const float mean = s * (1.f / 32.f);
    float m2v = 0.f;
#pragma unroll
    for (int j = 0; j < 8; ++j) { float d = hv[j] - mean; m2v += d * d; }
    m2v += __shfl_xor(m2v, 16); m2v += __shfl_xor(m2v, 32);
    if (g == 0) st8_sc1(rs, WS_HST + (unsigned)(((tilef * 16 + 2 * ct + khf) * 16 + r) * 8), (u32x2){__float_as_uint(mean), __float_as_uint(m2v)});
  }
  asm volatile("" : "+v"(hb.x), "+v"(hb.y), "+v"(hb.z), "+v"(hb.w), "+v"(xo_off));
  TS(34);
  WAIT_VM(0);
  TS(35);
  bar();
  if (tid == 0) {
    st8_sc1(rs, WS_F3 + (unsigned)((P * 16 + C) * 8), (u32x2){tlo, thi});
    if (bid == 0) st8_sc1(rs, WS_E, (u32x2){tlo, thi});
  }
  TS(8);
  const int m3 = w >> 1, kh3 = w & 1;
  unsigned rd7[7];
  {
    const int rho0 = 4 * g + q;
#pragma unroll
    for (int t = 0; t < 4; ++t) rd7[t] = rd4[t];
#pragma unroll
    for (int b = 0; b < 2; ++b) rd7[4 + b] = 4096u + (unsigned)(64 * rho0 + 16 * (pp ^ ((rho0 >> 2) & 1)) + 8 * (b ^ (pp & 1)));
    rd7[6] = 6144u + (unsigned)(32 * rho0 + 8 * pp);
  }
  f32x4 cs[7];
  {
    if (w < 7) {
      const unsigned a0 = w == 0 ? rd7[0] : w == 1 ? rd7[1] : w == 2 ? rd7[2] : w == 3 ? rd7[3] : w == 4 ? rd7[4] : w == 5 ? rd7[5] : rd7[6];
      const unsigned a1 = a0 + (w < 4 ? 2048u : w < 6 ? 1024u : 512u);
      f32x4 c0 = zero4, c1 = zero4;
#pragma unroll
      for (int i = 0; i < 16; i += 2) {
        c0 = mfma32(tr_frag(lds, a0 + i * 7168, a1 + i * 7168), ones, c0);
        c1 = mfma32(tr_frag(lds, a0 + (i + 1) * 7168, a1 + (i + 1) * 7168), ones, c1);
      }
      if (r == 0) *(LAS f32x4*)(lds + L_SP + w * 64 + g * 16) = c0 + c1;
    }
    bar();
#pragma unroll
    for (int t = 0; t < 7; ++t) cs[t] = *(LAS f32x4*)(lds + L_SP + t * 64 + g * 16);
    bar();
    TS(36);
  }
  const unsigned w2col = (unsigned)(ct * 128);
  auto issue_w2 = [&](int kqq, int il, int b) {
    const int rho = b * 8 + (lane >> 3);
    glds16(w2, (unsigned)(w2_row(kqq, il, rho) * 1024) + w2col + (unsigned)(((lane & 7) ^ ((rho >> 1) & 3)) * 16), ldsb + w2_slot(kqq, il) * 4096 + b * 1024);
  };
#pragma unroll
  for (int il = 0; il < 3; ++il) { issue_w2(kq, il, 2 * m2); issue_w2(kq, il, 2 * m2 + 1); }
  const int tile3 = P * 8 + 2 * m3;
  const int tilef3 = tile3 + kh3;
  wait_flags(ws, WS_F3 + (unsigned)((P * 16 + (m3 >> 1) * 8 + (lane & 7)) * 8), tlo, thi);
  TS(9);
  {
    u32x4 hf[2][8];
#pragma unroll
    for (int i = 0; i < 8; ++i)
#pragma unroll
      for (int tt = 0; tt < 2; ++tt) hf[tt][i] = ld16_sc1(rs, (unsigned)(lane * 16 + (i & 3) * 1024), WS_H + (unsigned)(((tile3 + tt) * 16 + kh3 * 8 + (i & 4)) * 1024));
    u32x2 st[4];
#pragma unroll
    for (int i = 0; i < 4; ++i) st[i] = ld8_sc1(rs, WS_HST + (unsigned)(((tilef3 * 16 + 4 * g + i) * 16 + r) * 8));
    f32x4 acc[2][7];
#pragma unroll
    for (int t = 0; t < 7; ++t) acc[0][t] = acc[1][t] = zero4;
    s16x8 f[2][7];
    u32x4 bA, bB, bC; u32x2 bD;
    const unsigned kbase = (unsigned)(kh3 * 8 * 7168);
#pragma unroll
    for (int i = 0; i <= 8; ++i) {
      if (i < 8) {
#pragma unroll
        for (int t = 0; t < 7; ++t) { const unsigned a = kbase + i * 7168 + rd7[t]; f[i & 1][t] = tr_frag(lds, a, a + (t < 4 ? 2048 : t < 6 ? 1024 : 512)); }
        SCHED_FENCE();
      }
      if (i > 0) {
        const int i0 = i - 1;
#pragma unroll
        for (int t = 0; t < 7; ++t) {
          acc[0][t] = mfma32(f[i0 & 1][t], as_frag(hf[0][i0]), acc[0][t]);
          acc[1][t] = mfma32(f[i0 & 1][t], as_frag(hf[1][i0]), acc[1][t]);
        }
        SCHED_FENCE();
      }
      if (i == 4) {
#pragma unroll
        for (int k = 4; k < 8; ++k) asm volatile("" : "+v"(hf[0][k]), "+v"(hf[1][k]));
        asm volatile("" : "+v"(st[0]), "+v"(st[1]), "+v"(st[2]), "+v"(st[3]));
        asm volatile("" ::: "memory");
        TS(37);
        __builtin_amdgcn_s_barrier();
        asm volatile("" ::: "memory");
        {
          const unsigned bo = (unsigned)((C * 112 + 16 * g) * 2), bo2 = (unsigned)((C * 112 + 64 + 8 * g) * 2), bo3 = (unsigned)((C * 112 + 96 + 4 * g) * 2);
          asm volatile("global_load_dwordx4 %0, %1, %2" : "=v"(bA) : "v"(bo), "s"(b1) : "memory");
          asm volatile("global_load_dwordx4 %0, %1, %2 offset:16" : "=v"(bB) : "v"(bo), "s"(b1) : "memory");
          asm volatile("global_load_dwordx4 %0, %1, %2" : "=v"(bC) : "v"(bo2), "s"(b1) : "memory");
          asm volatile("global_load_dwordx2 %0, %1, %2" : "=v"(bD) : "v"(bo3), "s"(b1) : "memory");
        }
#pragma unroll
        for (int il = 3; il < 6; ++il) { issue_w2(kq, il, 2 * m2); issue_w2(kq, il, 2 * m2 + 1); }
        issue_w2(w >> 2, 6, w & 3);
        SCHED_FENCE();
      }
    }
    TS(38);
    WAIT_VM(7);
    asm volatile("" : "+v"(bA), "+v"(bB), "+v"(bC), "+v"(bD));
    bar();
    TS(10);
    const unsigned ex_own = (unsigned)((w < 4 ? 28672 : 86016 - 4 * 7168) + w * 7168), ex_oth = (unsigned)((w < 4 ? 28672 : 86016 - 4 * 7168) + (w ^ 1) * 7168);
#pragma unroll
    for (int t = 0; t < 7; ++t) *(LAS f32x4*)(lds + ex_own + t * 1024 + lane * 16) = kh3 ? acc[0][t] : acc[1][t];
    float ms = (__uint_as_float(st[0].x) + __uint_as_float(st[1].x)) + (__uint_as_float(st[2].x) + __uint_as_float(st[3].x));
    ms += __shfl_xor(ms, 16); ms += __shfl_xor(ms, 32);
    const float mean = ms * (1.f / 16.f);
    float m2v = 0.f;
#pragma unroll
    for (int i = 0; i < 4; ++i) { float d = __uint_as_float(st[i].x) - mean; m2v += __uint_as_float(st[i].y) + 32.f * d * d; }
    m2v += __shfl_xor(m2v, 16); m2v += __shfl_xor(m2v, 32);
    const float var = m2v * (1.f / 512.f);
    const float r1 = rsqrtf(var + 1e-5f);
    const float rr = r1 * rsqrtf(var * r1 * r1 + 1e-5f);
    bar();
    TS(39);
    f32x4 v[7];
#pragma unroll
    for (int t = 0; t < 7; ++t) {
      v[t] = (kh3 ? acc[1][t] : acc[0][t]) + *(LAS f32x4*)(lds + ex_oth + t * 1024 + lane * 16);
      v[t] = (v[t] - mean * cs[t]) * rr;
    }
    const unsigned dst = WS_HH + (unsigned)((tilef3 * 16 + C) * 3584);
    const bool od = g & 1;
#pragma unroll
    for (int u = 0; u < 3; ++u) {
      const f32x4 A = od ? v[2 * u + 1] : v[2 * u], B = od ? v[2 * u] : v[2 * u + 1];
      const u32x4 bb = u == 0 ? bA : u == 1 ? bB : bC;
      u32x4 o;
      o.x = pk_bf16(gelu_tanh(A[0] + lo_f(bb.x)), gelu_tanh(A[1] + hi_f(bb.x)));
      o.y = pk_bf16(gelu_tanh(A[2] + lo_f(bb.y)), gelu_tanh(A[3] + hi_f(bb.y)));
      o.z = pk_bf16(gelu_tanh(B[0] + lo_f(bb.z)), gelu_tanh(B[1] + hi_f(bb.z)));
      o.w = pk_bf16(gelu_tanh(B[2] + lo_f(bb.w)), gelu_tanh(B[3] + hi_f(bb.w)));
      st16_sc1(rs, dst + u * 1024 + lane * 16, o);
    }
    {
      u32x2 o;
      o.x = pk_bf16(gelu_tanh(v[6][0] + lo_f(bD.x)), gelu_tanh(v[6][1] + hi_f(bD.x)));
      o.y = pk_bf16(gelu_tanh(v[6][2] + lo_f(bD.y)), gelu_tanh(v[6][3] + hi_f(bD.y)));
      st8_sc1(rs, dst + 3072 + lane * 8, o);
    }
  }
  TS(40);
  WAIT_VM(0);
  TS(41);
  bar();
  if (tid == 0) st8_sc1(rs, WS_F4 + (unsigned)((P * 16 + C) * 8), (u32x2){tlo, thi});
  TS(11);
  {
#pragma unroll
    for (int il = 11; il < 14; ++il) { issue_w2(kq, il, 2 * m2); issue_w2(kq, il, 2 * m2 + 1); }
    issue_w2(2 + (w >> 2), 6, w & 3);
    s16x8 wr[4][4];
#pragma unroll
    for (int il = 0; il < 4; ++il)
#pragma unroll
      for (int t = 0; t < 4; ++t) { const unsigned a = (unsigned)(w2_slot(kq, il) * 4096) + rd4[t]; wr[il][t] = tr_frag(lds, a, a + 2048); }
    bar();
    TS(42);
#pragma unroll
    for (int il = 7; il < 11; ++il) { issue_w2(kq, il, 2 * m2); issue_w2(kq, il, 2 * m2 + 1); }
    const u32x4 bb = *(const u32x4*)(b2 + ct * 64 + 16 * g + 8 * khf);
    wait_flags(ws, WS_F4 + (unsigned)((P * 16 + 4 * kq + (lane & 3)) * 8), tlo, thi);
    TS(12);
    u32x4 af[2][14];
#pragma unroll
    for (int i = 0; i < 14; ++i)
#pragma unroll
      for (int tt = 0; tt < 2; ++tt) {
        const unsigned src = WS_HH + (unsigned)(((tile0 + tt) * 16 + 4 * kq) * 3584);
        if (i < 12) af[tt][i] = ld16_sc1(rs, (unsigned)((i % 3) * 1024 + lane * 16), src + (i / 3) * 3584);
        else {
          u32x2 a = ld8_sc1(rs, (unsigned)(3072 + lane * 8), src + (2 * (i - 12)) * 3584), b = ld8_sc1(rs, (unsigned)(3072 + lane * 8), src + (2 * (i - 12) + 1) * 3584);
          af[tt][i] = (u32x4){a.x, a.y, b.x, b.y};
        }
      }
    bar();
    TS(43);
    f32x4 acc[2][4];
#pragma unroll
    for (int t = 0; t < 4; ++t) acc[0][t] = acc[1][t] = zero4;
    s16x8 f[2][4];
#pragma unroll
    for (int i = 0; i < 14; ++i) {
      if (i + 1 >= 4 && i + 1 < 14) {
#pragma unroll
        for (int t = 0; t < 4; ++t) { const unsigned a = (unsigned)(w2_slot(kq, i + 1) * 4096) + rd4[t]; f[(i + 1) & 1][t] = tr_frag(lds, a, a + 2048); }
        SCHED_FENCE();
      }
#pragma unroll
      for (int t = 0; t < 4; ++t) {
        const s16x8 wa = i < 4 ? wr[i & 3][t] : f[i & 1][t];
        acc[0][t] = mfma32(wa, as_frag(af[0][i]), acc[0][t]);
        acc[1][t] = mfma32(wa, as_frag(af[1][i]), acc[1][t]);
      }
      SCHED_FENCE();
    }
    TS(44);
    bar();
    TS(13);
    f32x4 k0 = zero4, k1 = zero4;
#pragma unroll
    for (int d = 0; d < 4; ++d) {
      if (d == kq) { k0 = acc[d >> 1][2 * (d & 1)]; k1 = acc[d >> 1][2 * (d & 1) + 1]; }
      else {
        LAS char* dst = lds + ((m2 * 4 + d) * 3 + ((kq - d - 1) & 3)) * 2048 + lane * 16;
        *(LAS f32x4*)dst = acc[d >> 1][2 * (d & 1)];
        *(LAS f32x4*)(dst + 1024) = acc[d >> 1][2 * (d & 1) + 1];
      }
    }
    bar();
#pragma unroll
    for (int s = 0; s < 3; ++s) {
      k0 += *(LAS f32x4*)(lds + (w * 3 + s) * 2048 + lane * 16);
      k1 += *(LAS f32x4*)(lds + (w * 3 + s) * 2048 + 1024 + lane * 16);
    }
    const f32x4 A = (g & 1) ? k1 : k0, B = (g & 1) ? k0 : k1;
    u32x4 o;
    o.x = pk_bf16(lo_f(hb.x) + A[0] + lo_f(bb.x), hi_f(hb.x) + A[1] + hi_f(bb.x));
    o.y = pk_bf16(lo_f(hb.y) + A[2] + lo_f(bb.y), hi_f(hb.y) + A[3] + hi_f(bb.y));
    o.z = pk_bf16(lo_f(hb.z) + B[0] + lo_f(bb.z), hi_f(hb.z) + B[1] + hi_f(bb.z));
    o.w = pk_bf16(lo_f(hb.w) + B[2] + lo_f(bb.w), hi_f(hb.w) + B[3] + hi_f(bb.w));
    *(u32x4*)((char*)out + xo_off) = o;
  }
  TS(14);
}

extern "C" void kernel_launch(void* const* d_in, const int* in_sizes, int n_in, void* d_out, int out_size, void* d_ws, size_t ws_size, hipStream_t stream) {
  const bf16_t* x = (const bf16_t*)d_in[0]; const bf16_t* wqkv = (const bf16_t*)d_in[1]; const bf16_t* wo = (const bf16_t*)d_in[2];
  const bf16_t* w1 = (const bf16_t*)d_in[4]; const bf16_t* b1 = (const bf16_t*)d_in[5]; const bf16_t* w2 = (const bf16_t*)d_in[6]; const bf16_t* b2 = (const bf16_t*)d_in[7];
  (void)hipFuncSetAttribute((const void*)fused_block, hipFuncAttributeMaxDynamicSharedMemorySize, 163840);
  fused_block<<<256, 512, 163840, stream>>>(x, wqkv, wo, w1, b1, w2, b2, (char*)d_ws, (bf16_t*)d_out);
}
```

```cpp
#include <hip/hip_runtime.h>
#include <stdint.h>

typedef unsigned short bf16_t;
typedef unsigned long long u64;
typedef __attribute__((ext_vector_type(8))) short s16x8;
typedef __attribute__((ext_vector_type(4))) short s16x4;
typedef __attribute__((ext_vector_type(4))) float f32x4;
typedef __attribute__((ext_vector_type(2))) float f32x2;
typedef __attribute__((ext_vector_type(4))) unsigned u32x4;
typedef __attribute__((ext_vector_type(2))) unsigned u32x2;
#define LAS __attribute__((address_space(3)))
#define DI __device__ __forceinline__

constexpr unsigned WS_E    = 0;
constexpr unsigned WS_F1   = 4096;
constexpr unsigned WS_F2   = 8192 + 2048;
constexpr unsigned WS_F3   = 8192 + 4096;
constexpr unsigned WS_F4   = 8192 + 6144;
constexpr unsigned WS_KVP  = 65536;
constexpr unsigned WS_HST  = 655360;
constexpr unsigned WS_O    = 1u << 20;
constexpr unsigned WS_H    = 3u << 20;
constexpr unsigned WS_HH   = 5u << 20;
constexpr int KVP_STRIDE = 2176;

typedef __attribute__((ext_vector_type(2))) __bf16 bf16x2_t;
DI unsigned pk_bf16(float lo, float hi) { return __builtin_bit_cast(unsigned, __builtin_convertvector((f32x2){lo, hi}, bf16x2_t)); }
DI float lo_f(unsigned u) { return __uint_as_float(u << 16); }
DI float hi_f(unsigned u) { return __uint_as_float(u & 0xffff0000u); }

DI void glds16(const void* sbase, unsigned voff, unsigned lds_dst) {
  unsigned keep;
  asm volatile("s_mov_b32 %0, m0\n\ts_mov_b32 m0, %3\n\ts_nop 0\n\tglobal_load_lds_dwordx4 %1, %2\n\ts_mov_b32 m0, %0"
               : "=&s"(keep) : "v"(voff), "s"(sbase), "s"(lds_dst) : "memory");
}
#define WAIT_VM(n) asm volatile("s_waitcnt vmcnt(" #n ")" ::: "memory")
DI void bar() { asm volatile("s_waitcnt lgkmcnt(0)" ::: "memory"); __builtin_amdgcn_s_barrier(); asm volatile("" ::: "memory"); }
#define SCHED_FENCE() __builtin_amdgcn_sched_barrier(0)
#define RAW_BAR() do { asm volatile("" ::: "memory"); __builtin_amdgcn_s_barrier(); asm volatile("" ::: "memory"); } while (0)
#define ILV(n) do { _Pragma("unroll") for (int z_ = 0; z_ < (n); ++z_) { __builtin_amdgcn_sched_group_barrier(0x008, 1, 0); __builtin_amdgcn_sched_group_barrier(0x100, 1, 0); } } while (0)
#define ILV2(n) do { _Pragma("unroll") for (int z_ = 0; z_ < (n); ++z_) { __builtin_amdgcn_sched_group_barrier(0x008, 1, 0); __builtin_amdgcn_sched_group_barrier(0x100, 2, 0); } } while (0)

DI s16x8 tr_frag(LAS char* lds, unsigned a0, unsigned a1) {
  s16x4 a = __builtin_amdgcn_ds_read_tr16_b64_v4i16((LAS s16x4*)(lds + a0));
  s16x4 b = __builtin_amdgcn_ds_read_tr16_b64_v4i16((LAS s16x4*)(lds + a1));
  return __builtin_shufflevector(a, b, 0, 1, 2, 3, 4, 5, 6, 7);
}
DI f32x4 mfma32(s16x8 a, s16x8 b, f32x4 c) { return __builtin_amdgcn_mfma_f32_16x16x32_bf16(a, b, c, 0, 0, 0); }
DI f32x4 mfma16(s16x4 a, s16x4 b, f32x4 c) { return __builtin_amdgcn_mfma_f32_16x16x16bf16_1k(a, b, c, 0, 0, 0); }
DI float phi(float t) { return (t > 0.f ? t + 1.f : __expf(t)) + 1e-6f; }
DI s16x8 as_frag(u32x4 v) { return __builtin_bit_cast(s16x8, v); }

typedef __amdgpu_buffer_rsrc_t rsrc_t;
#define XAUX 16
DI u32x4 ld16_sc1(rsrc_t rs, unsigned off) { return __builtin_amdgcn_raw_buffer_load_b128(rs, off, 0, 16); }
DI u32x4 ld16_sc1(rsrc_t rs, unsigned off, unsigned soff) { return __builtin_amdgcn_raw_buffer_load_b128(rs, off, soff, XAUX); }
DI u32x2 ld8_sc1(rsrc_t rs, unsigned off, unsigned soff = 0) { return __builtin_amdgcn_raw_buffer_load_b64(rs, off, soff, 16); }
DI void st16_sc1(rsrc_t rs, unsigned off, u32x4 v) { __builtin_amdgcn_raw_buffer_store_b128(v, rs, off, 0, 16); }
DI void st8_sc1(rsrc_t rs, unsigned off, u32x2 v) { __builtin_amdgcn_raw_buffer_store_b64(v, rs, off, 0, 16); }

DI void wait_flags(const char* ws, unsigned off, unsigned tlo, unsigned thi) {
  for (int it = 0; it < (1 << 18); ++it) {
    u32x2 v;
    asm volatile("global_load_dwordx2 %0, %1, %2 sc1\n\ts_waitcnt vmcnt(0)" : "=v"(v) : "v"(off), "s"(ws) : "memory");
    const bool ok = (v.x == tlo) & (v.y == thi);
    if (__builtin_amdgcn_ballot_w64(ok) == ~0ull) break;
    __builtin_amdgcn_s_sleep(1);
  }
  asm volatile("" ::: "memory");
}

#define TS(i)

DI unsigned g64_rd(int g, int q, int pp, int u, int b) {
  int rho = 4 * g + q;
  return (unsigned)(128 * rho + 16 * ((2 * pp + u) ^ ((rho >> 1) & 3)) + 8 * (b ^ (pp & 1)));
}
DI float gelu_tanh(float v) {
  float t = v * v;
  float a = v * (-2.3022082f - 0.1029432397f * t);
  float e = __builtin_amdgcn_exp2f(a);
  return v * __builtin_amdgcn_rcpf(1.f + e);
}
DI int w2_row(int kq, int i, int rho) {
  const int gg = (rho >> 2) & 3, j = 4 * (rho >> 4) + (rho & 3);
  if (i < 12) { int cp = 4 * kq + i / 3, u = i % 3; return u < 2 ? 112 * cp + 16 * gg + 8 * u + j : 112 * cp + 64 + 8 * gg + j; }
  int cp = 4 * kq + 2 * (i - 12) + (j >> 2);
  return 112 * cp + 96 + 4 * gg + (j & 3);
}
DI int slotB(int j) { return j < 7 ? j : j + 7; }
DI int slotC(int j) { return j < 7 ? 7 + j : 14 + j; }
DI int w2_slot(int kq, int il) {
  switch (il) {
    case 0: case 7: return 28 + kq;
    case 1: case 8: return 32 + kq;
    case 2: case 9: return 36 + kq;
    case 3: case 10: return slotB(kq);
    case 4: return slotB(4 + kq);
    case 5: return slotB(8 + kq);
    case 6: return kq < 2 ? slotB(12 + kq) : slotC(10 + kq);
    case 11: return slotC(kq);
    case 12: return slotC(4 + kq);
    default: return slotC(8 + kq);
  }
}

__global__ __launch_bounds__(512, 2) void fused_block(const bf16_t* __restrict__ x, const bf16_t* __restrict__ wqkv, const bf16_t* __restrict__ wo,
                                                      const bf16_t* __restrict__ w1, const bf16_t* __restrict__ b1, const bf16_t* __restrict__ w2,
                                                      const bf16_t* __restrict__ b2, char* ws, bf16_t* __restrict__ out) {
  extern __shared__ __attribute__((aligned(16))) char smem_[];
  LAS char* lds = (LAS char*)smem_;
  const int tid = threadIdx.x, lane = tid & 63;
  const int w = __builtin_amdgcn_readfirstlane(tid >> 6);
  const int bid = blockIdx.x;
  const int P = (bid & 7) * 2 + (bid >> 7), C = (bid >> 3) & 15;
  const int g = lane >> 4, r = lane & 15, q = (lane >> 2) & 3, pp = lane & 3;
  const unsigned ldsb = (unsigned)(size_t)smem_;
  const rsrc_t rs = __builtin_amdgcn_make_buffer_rsrc(ws, 0, 0x7fffffff, 0x00020000);
  TS(0);
  u32x2 Ev;
  asm volatile("global_load_dwordx2 %0, %1, %2 sc1" : "=v"(Ev) : "v"(WS_E), "s"(ws) : "memory");

  s16x8 ones; for (int j = 0; j < 8; ++j) ones[j] = (short)0x3f80;
  const f32x4 zero4 = (f32x4){0, 0, 0, 0};
  const int ct = C & 7, th = C >> 3;

  auto issue_w1p = [&](int kb, int pc) {
    auto srow = [&](int rho) { return 64 * (kb >> 1) + 16 * ((rho >> 2) & 3) + 8 * (kb & 1) + 4 * (rho >> 4) + (rho & 3); };
    const unsigned dst = ldsb + kb * 7168 + pc * 1024;
    if (pc < 4) { int rho = pc * 8 + (lane >> 3); glds16(w1, (unsigned)(srow(rho) * 3584 + C * 224 + (((lane & 7) ^ ((rho >> 1) & 3)) * 16)), dst); }
    else if (pc < 6) { int rho = (pc - 4) * 16 + (lane >> 2); glds16(w1, (unsigned)(srow(rho) * 3584 + C * 224 + 128 + (((lane & 3) ^ ((rho >> 2) & 1)) * 16)), dst); }
    else { int rho = lane >> 1; glds16(w1, (unsigned)(srow(rho) * 3584 + C * 224 + 192 + (lane & 1) * 16), dst); }
  };
  constexpr unsigned L_WKV = 0, L_WQ = 65536, L_X = 98304, L_S1 = 50176, L_KVS = 53248, L_CSQ = 62464;
  const int m1 = w >> 1, n1 = w & 1;
  u32x4 pq;
  unsigned tlo, thi;
  {
    const int xrow = lane >> 3;
    const unsigned xoff = (unsigned)(((P * 128 + w * 16 + xrow) * 512) * 2 + (((lane & 7) ^ (xrow & 7)) * 16));
    const int wk = lane >> 2;
    const unsigned wcol = (unsigned)((((((lane >> 1) & 1) ^ ((wk >> 3) & 1)) * 16) + (lane & 1) * 8) * 2);
    const unsigned woff = (unsigned)(wk * 1536 * 2) + wcol + (unsigned)(C * 64);
    auto issue_x = [&](int ch) {
      unsigned dst = ldsb + L_X + (ch & 3) * 16384 + w * 2048;
      glds16(x, xoff + ch * 128, dst);
      glds16(x, xoff + ch * 128 + 8 * 1024, dst + 1024);
    };
    auto issue_wkv = [&](int ch) {
      int sub = w >> 1, seg = w & 1;
      glds16(wqkv, woff + (unsigned)((ch * 64 + sub * 16) * 3072 + (1 + seg) * 1024), ldsb + L_WKV + ch * 8192 + w * 1024);
    };
    auto issue_wq = [&](int i) {
      int blk = w * 4 + i;
      glds16(wqkv, woff + (unsigned)(blk * 16 * 3072), ldsb + L_WQ + blk * 1024);
    };
    issue_x(0); issue_wkv(0);
    RAW_BAR();
    issue_x(1); issue_wkv(1);
    RAW_BAR();
    issue_x(2); issue_wkv(2); issue_wkv(3);

    TS(53);
    const unsigned xrd_own = L_X + w * 2048 + r * 128, xrd_oth = L_X + (w ^ 1) * 2048 + r * 128;
    const unsigned xsw0 = (unsigned)(((0 + g) ^ (r & 7)) * 16), xsw1 = (unsigned)(((4 + g) ^ (r & 7)) * 16);
    const unsigned wrd = (unsigned)((g >> 1) * 1024 + (8 * (g & 1) + q) * 64 + 8 * pp);
    const unsigned wrd_n = L_WKV + (g >> 1) * 1024 + wrd + 32u * (n1 ^ (g & 1));

    s16x8 xf[16];
    s16x8 xo[2][2], wf[2][2][2];
    f32x4 acc[2][2];
    f32x4 cs[2], sx = zero4, gx = zero4;
    acc[0][0] = acc[0][1] = acc[1][0] = acc[1][1] = cs[0] = cs[1] = zero4;
#pragma unroll
    for (int ch = 0; ch <= 8; ++ch) {
      if (ch < 8) {
        if (ch == 5) WAIT_VM(8); else if (ch == 7) WAIT_VM(4); else WAIT_VM(7);
        bar();
        if (ch == 0) TS(45); if (ch == 1) TS(46); if (ch == 2) TS(47); if (ch == 3) TS(48); if (ch == 4) TS(49); if (ch == 5) TS(50); if (ch == 6) TS(51); if (ch == 7) TS(52);
        if (ch <= 4) issue_x(ch + 3);
        if (ch <= 3) issue_wkv(ch + 4);
        if (ch == 4) { issue_wq(0); issue_wq(1); }
        if (ch == 5) { issue_wq(2); issue_wq(3); }
        SCHED_FENCE();
#pragma unroll
        for (int s = 0; s < 2; ++s) {
#pragma unroll
          for (int seg = 0; seg < 2; ++seg) {
            const unsigned a = wrd_n + ch * 8192 + s * 4096 + seg * 1024;
            wf[ch & 1][s][seg] = tr_frag(lds, a, a + 256);
          }
          xf[ch * 2 + s] = *(LAS s16x8*)(lds + xrd_own + (ch & 3) * 16384 + (s ? xsw1 : xsw0));
          xo[ch & 1][s] = *(LAS s16x8*)(lds + xrd_oth + (ch & 3) * 16384 + (s ? xsw1 : xsw0));
        }
      }
      if (ch > 0) {
        const int c0 = ch - 1;
#pragma unroll
        for (int s = 0; s < 2; ++s) {
          const s16x8 xa = xf[c0 * 2 + s], xb = xo[c0 & 1][s];
          acc[0][0] = mfma32(xa, wf[c0 & 1][s][0], acc[0][0]);
          acc[0][1] = mfma32(xa, wf[c0 & 1][s][1], acc[0][1]);
          acc[1][0] = mfma32(xb, wf[c0 & 1][s][0], acc[1][0]);
          acc[1][1] = mfma32(xb, wf[c0 & 1][s][1], acc[1][1]);
          sx = mfma32(xa, ones, sx);
          gx = mfma32(xa, xa, gx);
        }
        if (ch < 8) ILV(12);
        SCHED_FENCE();
        if ((c0 >> 1) == m1) {
#pragma unroll
          for (int s = 0; s < 2; ++s) {
            cs[0] = mfma32(ones, wf[c0 & 1][s][0], cs[0]);
            cs[1] = mfma32(ones, wf[c0 & 1][s][1], cs[1]);
          }
        }
      }
      SCHED_FENCE();
    }
    TS(1);
    {
      LAS float* sc = (LAS float*)(lds + L_S1 + w * 256);
      if (g == 0) { sc[r] = cs[0][0]; sc[16 + r] = cs[1][0]; }
      if (r == 0) *(LAS f32x4*)(sc + 32 + 4 * g) = sx;
      if ((r >> 2) == g) { float d = (r & 3) == 0 ? gx[0] : (r & 3) == 1 ? gx[1] : (r & 3) == 2 ? gx[2] : gx[3]; sc[48 + r] = d; }
    }
    bar();
    TS(20);
    TS(21);
    float mean1, rstd1;
    {
      float ck = 0.f, cv = 0.f;
#pragma unroll
      for (int mm = 0; mm < 4; ++mm) { LAS float* sc = (LAS float*)(lds + L_S1 + (2 * mm + n1) * 256); ck += sc[r]; cv += sc[16 + r]; }
      float pk[8], vv[8];
#pragma unroll
      for (int t = 0; t < 2; ++t) {
        LAS float* sc = (LAS float*)(lds + L_S1 + (w ^ t) * 256);
        f32x4 S = *(LAS f32x4*)(sc + 32 + 4 * g), Q = *(LAS f32x4*)(sc + 48 + 4 * g);
#pragma unroll
        for (int j = 0; j < 4; ++j) {
          float mu = S[j] * (1.f / 512.f); float var = fmaxf(Q[j] * (1.f / 512.f) - mu * mu, 0.f); float rsd = rsqrtf(var + 1e-5f);
          pk[t * 4 + j] = phi(rsd * (acc[t][0][j] - mu * ck));
          vv[t * 4 + j] = rsd * (acc[t][1][j] - mu * cv);
        }
      }
      {
        LAS float* sc = (LAS float*)(lds + L_S1 + w * 256);
        float mu = sc[32 + r] * (1.f / 512.f); float var = fmaxf(sc[48 + r] * (1.f / 512.f) - mu * mu, 0.f); mean1 = mu; rstd1 = rsqrtf(var + 1e-5f);
      }
      u32x4 pk8, v8;
      pk8.x = pk_bf16(pk[0], pk[1]); pk8.y = pk_bf16(pk[2], pk[3]); pk8.z = pk_bf16(pk[4], pk[5]); pk8.w = pk_bf16(pk[6], pk[7]);
      v8.x = pk_bf16(vv[0], vv[1]); v8.y = pk_bf16(vv[2], vv[3]); v8.z = pk_bf16(vv[4], vv[5]); v8.w = pk_bf16(vv[6], vv[7]);
      f32x4 kvp = mfma32(as_frag(pk8), as_frag(v8), zero4);
      f32x4 zp = mfma32(as_frag(pk8), ones, zero4);
      LAS char* sc = lds + L_KVS + w * 1088;
      *(LAS f32x4*)(sc + lane * 16) = kvp;
      if (r == 0) *(LAS f32x4*)(sc + 1024 + g * 16) = zp;
    }
    TS(22);
    WAIT_VM(0);
    TS(23);
    bar();
    TS(24);
    asm volatile("" : "+v"(Ev));
    {
      const u64 E = ((u64)(unsigned)__builtin_amdgcn_readfirstlane(Ev.y) << 32) | (unsigned)__builtin_amdgcn_readfirstlane(Ev.x);
      const u64 T = E * 0x9E3779B97F4A7C15ull + 0xD1B54A32D192ED03ull;
      tlo = (unsigned)T; thi = (unsigned)(T >> 32);
    }
    if (w < 2) {
      f32x4 s = zero4, sz = zero4;
#pragma unroll
      for (int mm = 0; mm < 4; ++mm) {
        s += *(LAS f32x4*)(lds + L_KVS + (2 * mm + w) * 1088 + lane * 16);
        sz += *(LAS f32x4*)(lds + L_KVS + (2 * mm + w) * 1088 + 1024 + (lane & 3) * 16);
      }
      const unsigned dst = WS_KVP + (unsigned)((C * 16 + P) * KVP_STRIDE + w * 1088 + lane * 16);
      st16_sc1(rs, dst, __builtin_bit_cast(u32x4, s));
      if (lane < 4) st16_sc1(rs, dst + 1024, __builtin_bit_cast(u32x4, sz));
      WAIT_VM(0);
      if (lane == 0) st8_sc1(rs, WS_F1 + (unsigned)(((C * 16 + P) * 2 + w) * 8), (u32x2){tlo, thi});
    }
    TS(2);
    f32x4 aq[2];
    aq[0] = aq[1] = zero4;
    {
      const unsigned a0 = L_WQ + wrd + 32u * (0 ^ (g & 1)), a1 = L_WQ + wrd + 32u * (1 ^ (g & 1));
      const int worow = lane >> 3;
      const unsigned wooff = (unsigned)(worow * 1024 + ct * 128 + (((lane & 7) ^ ((worow >> 1) & 3)) * 16));
      s16x8 f[2][4];
#pragma unroll
      for (int kk = 0; kk <= 8; ++kk) {
        if (kk < 8) {
          if (w >= 4) {
#pragma unroll
            for (int i = 0; i < 2; ++i) { int blk = (w - 4) * 16 + kk * 2 + i; glds16(wo, wooff + blk * 8192, ldsb + L_X + blk * 1024); }
          } else if (kk < 7) issue_w1p(w, kk);
          SCHED_FENCE();
#pragma unroll
          for (int s = 0; s < 2; ++s) {
            f[kk & 1][s * 2 + 0] = tr_frag(lds, a0 + (kk * 2 + s) * 2048, a0 + (kk * 2 + s) * 2048 + 256);
            f[kk & 1][s * 2 + 1] = tr_frag(lds, a1 + (kk * 2 + s) * 2048, a1 + (kk * 2 + s) * 2048 + 256);
          }
        } else {
#pragma unroll
          for (int s = 0; s < 2; ++s) {
            f[kk & 1][s * 2 + 0] = tr_frag(lds, a0 + (w * 2 + s) * 2048, a0 + (w * 2 + s) * 2048 + 256);
            f[kk & 1][s * 2 + 1] = tr_frag(lds, a1 + (w * 2 + s) * 2048, a1 + (w * 2 + s) * 2048 + 256);
          }
        }
        if (kk > 0) {
          const int k0 = kk - 1;
#pragma unroll
          for (int s = 0; s < 2; ++s) {
            aq[0] = mfma32(f[k0 & 1][s * 2 + 0], xf[k0 * 2 + s], aq[0]);
            aq[1] = mfma32(f[k0 & 1][s * 2 + 1], xf[k0 * 2 + s], aq[1]);
          }
          ILV2(4);
        }
        SCHED_FENCE();
      }
      f32x4 c0 = mfma32(f[0][0], ones, zero4), c1 = mfma32(f[0][1], ones, zero4);
      c0 = mfma32(f[0][2], ones, c0); c1 = mfma32(f[0][3], ones, c1);
      if (r == 0) { *(LAS f32x4*)(lds + L_CSQ + w * 128 + g * 16) = c0; *(LAS f32x4*)(lds + L_CSQ + w * 128 + 64 + g * 16) = c1; }
    }
    TS(25);
    bar();
    TS(3);
    if (w >= 4) {
#pragma unroll
      for (int pc = 0; pc < 7; ++pc) issue_w1p(w, pc);
    }
    {
      f32x4 csq[2];
      csq[0] = csq[1] = zero4;
#pragma unroll
      for (int ww = 0; ww < 8; ++ww) { csq[0] += *(LAS f32x4*)(lds + L_CSQ + ww * 128 + g * 16); csq[1] += *(LAS f32x4*)(lds + L_CSQ + ww * 128 + 64 + g * 16); }
      float a[4], b[4];
#pragma unroll
      for (int j = 0; j < 4; ++j) { a[j] = phi(rstd1 * (aq[0][j] - mean1 * csq[0][j])); b[j] = phi(rstd1 * (aq[1][j] - mean1 * csq[1][j])); }
      pq.x = pk_bf16(a[0], a[1]); pq.y = pk_bf16(a[2], a[3]); pq.z = pk_bf16(b[0], b[1]); pq.w = pk_bf16(b[2], b[3]);
    }
  }
  const int m2 = w >> 2, kq = w & 3;
  unsigned rd4[4];
#pragma unroll
  for (int t = 0; t < 4; ++t) rd4[t] = g64_rd(g, q, pp, t >> 1, t & 1);
  TS(4);
  constexpr unsigned L_SP = 114688;
  constexpr unsigned L_H1 = 65536;
  wait_flags(ws, WS_F1 + (unsigned)(((C * 16 + 2 * w) * 2 + (lane & 3)) * 8), tlo, thi);
  TS(5);
  {
    const unsigned base = WS_KVP + (unsigned)((C * 16 + 2 * w) * KVP_STRIDE);
    u32x4 a[3], b[3];
#pragma unroll
    for (int rd = 0; rd < 3; ++rd) {
      const int i = rd < 2 ? rd * 64 + lane : 128 + (lane & 7);
      a[rd] = ld16_sc1(rs, base + i * 16);
      b[rd] = ld16_sc1(rs, base + KVP_STRIDE + i * 16);
    }
#pragma unroll
    for (int rd = 0; rd < 3; ++rd) {
      const int i = rd < 2 ? rd * 64 + lane : 128 + (lane & 7);
      *(LAS f32x4*)(lds + L_H1 + w * 2176 + i * 16) = __builtin_bit_cast(f32x4, a[rd]) + __builtin_bit_cast(f32x4, b[rd]);
    }
  }
  TS(29);
  bar();
  if (tid < 136) {
    f32x4 s = zero4;
#pragma unroll
    for (int ww = 0; ww < 8; ++ww) s += *(LAS f32x4*)(lds + L_H1 + ww * 2176 + tid * 16);
    *(LAS u32x2*)(lds + L_H1 + 17408 + tid * 8) = (u32x2){pk_bf16(s[0], s[1]), pk_bf16(s[2], s[3])};
  }
  bar();
  {
    u32x4 o;
#pragma unroll
    for (int hd = 0; hd < 2; ++hd) {
      s16x4 kvA = *(LAS s16x4*)(lds + L_H1 + 17408 + (hd * 68 + lane) * 8);
      s16x4 zA = *(LAS s16x4*)(lds + L_H1 + 17408 + (hd * 68 + 64 + g) * 8);
      u32x2 pu; pu.x = hd ? pq.z : pq.x; pu.y = hd ? pq.w : pq.y;
      s16x4 p4 = __builtin_bit_cast(s16x4, pu);
      f32x4 num = mfma16(kvA, p4, zero4);
      f32x4 den = mfma16(zA, p4, zero4);
      const float inv = 1.f / (den[0] + 1e-6f);
      if (hd == 0) { o.x = pk_bf16(num[0] * inv, num[1] * inv); o.y = pk_bf16(num[2] * inv, num[3] * inv); }
      else { o.z = pk_bf16(num[0] * inv, num[1] * inv); o.w = pk_bf16(num[2] * inv, num[3] * inv); }
    }
    st16_sc1(rs, WS_O + (unsigned)((((P * 8 + w) * 16 + C) * 64 + lane) * 16), o);
  }
  TS(30);
  WAIT_VM(0);
  TS(31);
  bar();
  if (tid == 0) st8_sc1(rs, WS_F2 + (unsigned)((P * 16 + C) * 8), (u32x2){tlo, thi});
  TS(6);
  s16x8 wof[4][4];
#pragma unroll
  for (int i = 0; i < 4; ++i)
#pragma unroll
    for (int t = 0; t < 4; ++t) { const unsigned a = L_X + (unsigned)((kq * 4 + i) * 4096) + rd4[t]; wof[i][t] = tr_frag(lds, a, a + 2048); }
  bar();
  TS(28);
#pragma unroll
  for (int pc = 0; pc < 7; ++pc) issue_w1p(8 + w, pc);
  const int tile0 = P * 8 + th * 4 + 2 * m2;
  const int tilef = tile0 + (kq >> 1), khf = kq & 1;
  unsigned xo_off = (unsigned)(((tilef * 16 + r) * 512 + ct * 64 + 16 * g + 8 * khf) * 2);
  const u32x4 xr = *(const u32x4*)((const char*)x + xo_off);
  wait_flags(ws, WS_F2 + (unsigned)((P * 16 + 4 * kq + (lane & 3)) * 8), tlo, thi);
  TS(7);
  u32x4 hb;
  {
    u32x4 of[2][4];
#pragma unroll
    for (int i = 0; i < 4; ++i)
#pragma unroll
      for (int tt = 0; tt < 2; ++tt) of[tt][i] = ld16_sc1(rs, (unsigned)(lane * 16 + i * 1024), WS_O + (unsigned)(((tile0 + tt) * 16 + kq * 4) * 1024));
    SCHED_FENCE();
    f32x4 acc[2][4];
#pragma unroll
    for (int t = 0; t < 4; ++t) acc[0][t] = acc[1][t] = zero4;
#pragma unroll
    for (int i = 0; i < 4; ++i)
#pragma unroll
      for (int tt = 0; tt < 2; ++tt)
#pragma unroll
        for (int t = 0; t < 4; ++t) acc[tt][t] = mfma32(wof[i][t], as_frag(of[tt][i]), acc[tt][t]);
    f32x4 k0 = zero4, k1 = zero4;
#pragma unroll
    for (int d = 0; d < 4; ++d) {
      if (d == kq) { k0 = acc[d >> 1][2 * (d & 1)]; k1 = acc[d >> 1][2 * (d & 1) + 1]; }
      else {
        LAS char* dst = lds + L_SP + ((m2 * 4 + d) * 3 + ((kq - d - 1) & 3)) * 2048 + lane * 16;
        *(LAS f32x4*)dst = acc[d >> 1][2 * (d & 1)];
        *(LAS f32x4*)(dst + 1024) = acc[d >> 1][2 * (d & 1) + 1];
      }
    }
    TS(32);
    bar();
    TS(33);
#pragma unroll
    for (int s = 0; s < 3; ++s) {
      k0 += *(LAS f32x4*)(lds + L_SP + (w * 3 + s) * 2048 + lane * 16);
      k1 += *(LAS f32x4*)(lds + L_SP + (w * 3 + s) * 2048 + 1024 + lane * 16);
    }
    const f32x4 A = (g & 1) ? k1 : k0, B = (g & 1) ? k0 : k1;
    hb.x = pk_bf16(lo_f(xr.x) + A[0], hi_f(xr.x) + A[1]); hb.y = pk_bf16(lo_f(xr.y) + A[2], hi_f(xr.y) + A[3]);
    hb.z = pk_bf16(lo_f(xr.z) + B[0], hi_f(xr.z) + B[1]); hb.w = pk_bf16(lo_f(xr.w) + B[2], hi_f(xr.w) + B[3]);
    st16_sc1(rs, WS_H + (unsigned)(((tilef * 16 + 2 * ct + khf) * 64 + lane) * 16), hb);
    float hv[8] = {lo_f(hb.x), hi_f(hb.x), lo_f(hb.y), hi_f(hb.y), lo_f(hb.z), hi_f(hb.z), lo_f(hb.w), hi_f(hb.w)};
    float s = 0.f;
#pragma unroll
    for (int j = 0; j < 8; ++j) s += hv[j];
    s += __shfl_xor(s, 16); s += __shfl_xor(s, 32);
    const float mean = s * (1.f / 32.f);
    float m2v = 0.f;
#pragma unroll
    for (int j = 0; j < 8; ++j) { float d = hv[j] - mean; m2v += d * d; }
    m2v += __shfl_xor(m2v, 16); m2v += __shfl_xor(m2v, 32);
    if (g == 0) st8_sc1(rs, WS_HST + (unsigned)(((tilef * 16 + 2 * ct + khf) * 16 + r) * 8), (u32x2){__float_as_uint(mean), __float_as_uint(m2v)});
  }
  asm volatile("" : "+v"(hb.x), "+v"(hb.y), "+v"(hb.z), "+v"(hb.w), "+v"(xo_off));
  TS(34);
  WAIT_VM(0);
  TS(35);
  bar();
  if (tid == 0) {
    st8_sc1(rs, WS_F3 + (unsigned)((P * 16 + C) * 8), (u32x2){tlo, thi});
    if (bid == 0) st8_sc1(rs, WS_E, (u32x2){tlo, thi});
  }
  TS(8);
  const int m3 = w >> 1, kh3 = w & 1;
  unsigned rd7[7];
  {
    const int rho0 = 4 * g + q;
#pragma unroll
    for (int t = 0; t < 4; ++t) rd7[t] = rd4[t];
#pragma unroll
    for (int b = 0; b < 2; ++b) rd7[4 + b] = 4096u + (unsigned)(64 * rho0 + 16 * (pp ^ ((rho0 >> 2) & 1)) + 8 * (b ^ (pp & 1)));
    rd7[6] = 6144u + (unsigned)(32 * rho0 + 8 * pp);
  }
  f32x4 cs[7];
  {
    if (w < 7) {
      const unsigned a0 = w == 0 ? rd7[0] : w == 1 ? rd7[1] : w == 2 ? rd7[2] : w == 3 ? rd7[3] : w == 4 ? rd7[4] : w == 5 ? rd7[5] : rd7[6];
      const unsigned a1 = a0 + (w < 4 ? 2048u : w < 6 ? 1024u : 512u);
      f32x4 c0 = zero4, c1 = zero4;
#pragma unroll
      for (int i = 0; i < 16; i += 2) {
        c0 = mfma32(tr_frag(lds, a0 + i * 7168, a1 + i * 7168), ones, c0);
        c1 = mfma32(tr_frag(lds, a0 + (i + 1) * 7168, a1 + (i + 1) * 7168), ones, c1);
      }
      if (r == 0) *(LAS f32x4*)(lds + L_SP + w * 64 + g * 16) = c0 + c1;
    }
    bar();
#pragma unroll
    for (int t = 0; t < 7; ++t) cs[t] = *(LAS f32x4*)(lds + L_SP + t * 64 + g * 16);
    bar();
    TS(36);
  }
  const unsigned w2col = (unsigned)(ct * 128);
  auto issue_w2 = [&](int kqq, int il, int b) {
    const int rho = b * 8 + (lane >> 3);
    glds16(w2, (unsigned)(w2_row(kqq, il, rho) * 1024) + w2col + (unsigned)(((lane & 7) ^ ((rho >> 1) & 3)) * 16), ldsb + w2_slot(kqq, il) * 4096 + b * 1024);
  };
#pragma unroll
  for (int il = 0; il < 3; ++il) { issue_w2(kq, il, 2 * m2); issue_w2(kq, il, 2 * m2 + 1); }
  const int tile3 = P * 8 + 2 * m3;
  const int tilef3 = tile3 + kh3;
  wait_flags(ws, WS_F3 + (unsigned)((P * 16 + (m3 >> 1) * 8 + (lane & 7)) * 8), tlo, thi);
  TS(9);
  {
    u32x4 hf[2][8];
    auto ld_h = [&](int i) {
#pragma unroll
      for (int tt = 0; tt < 2; ++tt) hf[tt][i] = ld16_sc1(rs, (unsigned)(lane * 16 + (i & 3) * 1024), WS_H + (unsigned)(((tile3 + tt) * 16 + kh3 * 8 + (i & 4)) * 1024));
    };
    ld_h(0); ld_h(1); ld_h(2);
    u32x2 st[4];
    f32x4 acc[2][7];
#pragma unroll
    for (int t = 0; t < 7; ++t) acc[0][t] = acc[1][t] = zero4;
    s16x8 f[2][7];
    u32x4 bA, bB, bC; u32x2 bD;
    const unsigned kbase = (unsigned)(kh3 * 8 * 7168);
#pragma unroll
    for (int i = 0; i <= 8; ++i) {
      if (i + 3 < 8) ld_h(i + 3);
      if (i == 5) {
#pragma unroll
        for (int k = 0; k < 4; ++k) st[k] = ld8_sc1(rs, WS_HST + (unsigned)(((tilef3 * 16 + 4 * g + k) * 16 + r) * 8));
      }
      SCHED_FENCE();
      if (i < 8) {
#pragma unroll
        for (int t = 0; t < 7; ++t) { const unsigned a = kbase + i * 7168 + rd7[t]; f[i & 1][t] = tr_frag(lds, a, a + (t < 4 ? 2048 : t < 6 ? 1024 : 512)); }
      }
      if (i > 0) {
        const int i0 = i - 1;
#pragma unroll
        for (int t = 0; t < 7; ++t) {
          acc[0][t] = mfma32(f[i0 & 1][t], as_frag(hf[0][i0]), acc[0][t]);
          acc[1][t] = mfma32(f[i0 & 1][t], as_frag(hf[1][i0]), acc[1][t]);
        }
        if (i < 8) ILV(14);
      }
      SCHED_FENCE();
      if (i == 7) {
        asm volatile("" : "+v"(hf[0][7]), "+v"(hf[1][7]));
        asm volatile("" : "+v"(st[0]), "+v"(st[1]), "+v"(st[2]), "+v"(st[3]));
        TS(37);
        RAW_BAR();
        {
          const unsigned bo = (unsigned)((C * 112 + 16 * g) * 2), bo2 = (unsigned)((C * 112 + 64 + 8 * g) * 2), bo3 = (unsigned)((C * 112 + 96 + 4 * g) * 2);
          asm volatile("global_load_dwordx4 %0, %1, %2" : "=v"(bA) : "v"(bo), "s"(b1) : "memory");
          asm volatile("global_load_dwordx4 %0, %1, %2 offset:16" : "=v"(bB) : "v"(bo), "s"(b1) : "memory");
          asm volatile("global_load_dwordx4 %0, %1, %2" : "=v"(bC) : "v"(bo2), "s"(b1) : "memory");
          asm volatile("global_load_dwordx2 %0, %1, %2" : "=v"(bD) : "v"(bo3), "s"(b1) : "memory");
        }
#pragma unroll
        for (int il = 3; il < 6; ++il) { issue_w2(kq, il, 2 * m2); issue_w2(kq, il, 2 * m2 + 1); }
        issue_w2(w >> 2, 6, w & 3);
        SCHED_FENCE();
      }
    }
    TS(38);
    WAIT_VM(7);
    asm volatile("" : "+v"(bA), "+v"(bB), "+v"(bC), "+v"(bD));
    bar();
    TS(10);
    const unsigned ex_own = (unsigned)((w < 4 ? 28672 : 86016 - 4 * 7168) + w * 7168), ex_oth = (unsigned)((w < 4 ? 28672 : 86016 - 4 * 7168) + (w ^ 1) * 7168);
#pragma unroll
    for (int t = 0; t < 7; ++t) *(LAS f32x4*)(lds + ex_own + t * 1024 + lane * 16) = kh3 ? acc[0][t] : acc[1][t];
    float ms = (__uint_as_float(st[0].x) + __uint_as_float(st[1].x)) + (__uint_as_float(st[2].x) + __uint_as_float(st[3].x));
    ms += __shfl_xor(ms, 16); ms += __shfl_xor(ms, 32);
    const float mean = ms * (1.f / 16.f);
    float m2v = 0.f;
#pragma unroll
    for (int i = 0; i < 4; ++i) { float d = __uint_as_float(st[i].x) - mean; m2v += __uint_as_float(st[i].y) + 32.f * d * d; }
    m2v += __shfl_xor(m2v, 16); m2v += __shfl_xor(m2v, 32);
    const float var = m2v * (1.f / 512.f);
    const float r1 = rsqrtf(var + 1e-5f);
    const float rr = r1 * rsqrtf(var * r1 * r1 + 1e-5f);
    bar();
    TS(39);
    f32x4 v[7];
#pragma unroll
    for (int t = 0; t < 7; ++t) {
      v[t] = (kh3 ? acc[1][t] : acc[0][t]) + *(LAS f32x4*)(lds + ex_oth + t * 1024 + lane * 16);
      v[t] = (v[t] - mean * cs[t]) * rr;
    }
    const unsigned dst = WS_HH + (unsigned)((tilef3 * 16 + C) * 3584);
    const bool od = g & 1;
#pragma unroll
    for (int u = 0; u < 3; ++u) {
      const f32x4 A = od ? v[2 * u + 1] : v[2 * u], B = od ? v[2 * u] : v[2 * u + 1];
      const u32x4 bb = u == 0 ? bA : u == 1 ? bB : bC;
      u32x4 o;
      o.x = pk_bf16(gelu_tanh(A[0] + lo_f(bb.x)), gelu_tanh(A[1] + hi_f(bb.x)));
      o.y = pk_bf16(gelu_tanh(A[2] + lo_f(bb.y)), gelu_tanh(A[3] + hi_f(bb.y)));
      o.z = pk_bf16(gelu_tanh(B[0] + lo_f(bb.z)), gelu_tanh(B[1] + hi_f(bb.z)));
      o.w = pk_bf16(gelu_tanh(B[2] + lo_f(bb.w)), gelu_tanh(B[3] + hi_f(bb.w)));
      st16_sc1(rs, dst + u * 1024 + lane * 16, o);
    }
    {
      u32x2 o;
      o.x = pk_bf16(gelu_tanh(v[6][0] + lo_f(bD.x)), gelu_tanh(v[6][1] + hi_f(bD.x)));
      o.y = pk_bf16(gelu_tanh(v[6][2] + lo_f(bD.y)), gelu_tanh(v[6][3] + hi_f(bD.y)));
      st8_sc1(rs, dst + 3072 + lane * 8, o);
    }
  }
  TS(40);
  WAIT_VM(0);
  TS(41);
  bar();
  if (tid == 0) st8_sc1(rs, WS_F4 + (unsigned)((P * 16 + C) * 8), (u32x2){tlo, thi});
  TS(11);
  {
#pragma unroll
    for (int il = 11; il < 14; ++il) { issue_w2(kq, il, 2 * m2); issue_w2(kq, il, 2 * m2 + 1); }
    issue_w2(2 + (w >> 2), 6, w & 3);
    s16x8 wr[4][4];
#pragma unroll
    for (int il = 0; il < 4; ++il)
#pragma unroll
      for (int t = 0; t < 4; ++t) { const unsigned a = (unsigned)(w2_slot(kq, il) * 4096) + rd4[t]; wr[il][t] = tr_frag(lds, a, a + 2048); }
    bar();
    TS(42);
#pragma unroll
    for (int il = 7; il < 11; ++il) { issue_w2(kq, il, 2 * m2); issue_w2(kq, il, 2 * m2 + 1); }
    const u32x4 bb = *(const u32x4*)(b2 + ct * 64 + 16 * g + 8 * khf);
    wait_flags(ws, WS_F4 + (unsigned)((P * 16 + 4 * kq + (lane & 3)) * 8), tlo, thi);
    TS(12);
    u32x4 af[2][14];
    auto ld_a = [&](int i) {
#pragma unroll
      for (int tt = 0; tt < 2; ++tt) {
        const unsigned src = WS_HH + (unsigned)(((tile0 + tt) * 16 + 4 * kq) * 3584);
        if (i < 12) af[tt][i] = ld16_sc1(rs, (unsigned)((i % 3) * 1024 + lane * 16), src + (i / 3) * 3584);
        else {
          u32x2 a = ld8_sc1(rs, (unsigned)(3072 + lane * 8), src + (2 * (i - 12)) * 3584), b = ld8_sc1(rs, (unsigned)(3072 + lane * 8), src + (2 * (i - 12) + 1) * 3584);
          af[tt][i] = (u32x4){a.x, a.y, b.x, b.y};
        }
      }
    };
    ld_a(0); ld_a(1); ld_a(2); ld_a(3);
    f32x4 acc[2][4];
#pragma unroll
    for (int t = 0; t < 4; ++t) acc[0][t] = acc[1][t] = zero4;
    s16x8 f[2][4];
#pragma unroll
    for (int i = 0; i < 14; ++i) {
      if (i + 4 < 14) ld_a(i + 4);
      if (i == 3) { RAW_BAR(); TS(43); }
      SCHED_FENCE();
      if (i + 1 >= 4 && i + 1 < 14) {
#pragma unroll
        for (int t = 0; t < 4; ++t) { const unsigned a = (unsigned)(w2_slot(kq, i + 1) * 4096) + rd4[t]; f[(i + 1) & 1][t] = tr_frag(lds, a, a + 2048); }
      }
#pragma unroll
      for (int t = 0; t < 4; ++t) {
        const s16x8 wa = i < 4 ? wr[i & 3][t] : f[i & 1][t];
        acc[0][t] = mfma32(wa, as_frag(af[0][i]), acc[0][t]);
        acc[1][t] = mfma32(wa, as_frag(af[1][i]), acc[1][t]);
      }
      if (i + 1 >= 4 && i + 1 < 14) ILV(8);
      SCHED_FENCE();
    }
    TS(44);
    bar();
    TS(13);
    f32x4 k0 = zero4, k1 = zero4;
#pragma unroll
    for (int d = 0; d < 4; ++d) {
      if (d == kq) { k0 = acc[d >> 1][2 * (d & 1)]; k1 = acc[d >> 1][2 * (d & 1) + 1]; }
      else {
        LAS char* dst = lds + ((m2 * 4 + d) * 3 + ((kq - d - 1) & 3)) * 2048 + lane * 16;
        *(LAS f32x4*)dst = acc[d >> 1][2 * (d & 1)];
        *(LAS f32x4*)(dst + 1024) = acc[d >> 1][2 * (d & 1) + 1];
      }
    }
    bar();
#pragma unroll
    for (int s = 0; s < 3; ++s) {
      k0 += *(LAS f32x4*)(lds + (w * 3 + s) * 2048 + lane * 16);
      k1 += *(LAS f32x4*)(lds + (w * 3 + s) * 2048 + 1024 + lane * 16);
    }
    const f32x4 A = (g & 1) ? k1 : k0, B = (g & 1) ? k0 : k1;
    u32x4 o;
    o.x = pk_bf16(lo_f(hb.x) + A[0] + lo_f(bb.x), hi_f(hb.x) + A[1] + hi_f(bb.x));
    o.y = pk_bf16(lo_f(hb.y) + A[2] + lo_f(bb.y), hi_f(hb.y) + A[3] + hi_f(bb.y));
    o.z = pk_bf16(lo_f(hb.z) + B[0] + lo_f(bb.z), hi_f(hb.z) + B[1] + hi_f(bb.z));
    o.w = pk_bf16(lo_f(hb.w) + B[2] + lo_f(bb.w), hi_f(hb.w) + B[3] + hi_f(bb.w));
    *(u32x4*)((char*)out + xo_off) = o;
  }
  TS(14);
}

extern "C" void kernel_launch(void* const* d_in, const int* in_sizes, int n_in, void* d_out, int out_size, void* d_ws, size_t ws_size, hipStream_t stream) {
  const bf16_t* x = (const bf16_t*)d_in[0]; const bf16_t* wqkv = (const bf16_t*)d_in[1]; const bf16_t* wo = (const bf16_t*)d_in[2];
  const bf16_t* w1 = (const bf16_t*)d_in[4]; const bf16_t* b1 = (const bf16_t*)d_in[5]; const bf16_t* w2 = (const bf16_t*)d_in[6]; const bf16_t* b2 = (const bf16_t*)d_in[7];
  (void)hipFuncSetAttribute((const void*)fused_block, hipFuncAttributeMaxDynamicSharedMemorySize, 163840);
  fused_block<<<256, 512, 163840, stream>>>(x, wqkv, wo, w1, b1, w2, b2, (char*)d_ws, (bf16_t*)d_out);
}
```

```cpp
#include <hip/hip_runtime.h>
#include <stdint.h>

typedef unsigned short bf16_t;
typedef unsigned long long u64;
typedef __attribute__((ext_vector_type(8))) short s16x8;
typedef __attribute__((ext_vector_type(4))) short s16x4;
typedef __attribute__((ext_vector_type(4))) float f32x4;
typedef __attribute__((ext_vector_type(2))) float f32x2;
typedef __attribute__((ext_vector_type(4))) unsigned u32x4;
typedef __attribute__((ext_vector_type(2))) unsigned u32x2;
#define LAS __attribute__((address_space(3)))
#define DI __device__ __forceinline__

constexpr unsigned WS_E    = 0;
constexpr unsigned WS_F1   = 65536;
constexpr unsigned WS_F2   = 262144;
constexpr unsigned WS_F3   = 524288;
constexpr unsigned WS_F4A  = 786432;
constexpr unsigned WS_F4B  = 1048576;
constexpr unsigned WS_KVP  = 1310720;
constexpr unsigned WS_O    = 2u << 20;
constexpr unsigned WS_H    = 4u << 20;
constexpr unsigned WS_HH   = 6u << 20;
constexpr unsigned WS_HS   = 14u << 20;
constexpr int KVP_STRIDE = 1152;

typedef __attribute__((ext_vector_type(2))) __bf16 bf16x2_t;
DI unsigned pk_bf16(float lo, float hi) { return __builtin_bit_cast(unsigned, __builtin_convertvector((f32x2){lo, hi}, bf16x2_t)); }
DI float lo_f(unsigned u) { return __uint_as_float(u << 16); }
DI float hi_f(unsigned u) { return __uint_as_float(u & 0xffff0000u); }

DI void glds16(const void* sbase, unsigned voff, unsigned lds_dst) {
  unsigned keep;
  asm volatile("s_mov_b32 %0, m0\n\ts_mov_b32 m0, %3\n\ts_nop 0\n\tglobal_load_lds_dwordx4 %1, %2\n\ts_mov_b32 m0, %0"
               : "=&s"(keep) : "v"(voff), "s"(sbase), "s"(lds_dst) : "memory");
}
#define GLD4(d, voff, sb, imm)  asm volatile("global_load_dwordx4 %0, %1, %2 offset:%3" : "=v"(d) : "v"(voff), "s"(sb), "i"(imm) : "memory")
#define GLD2(d, voff, sb, imm)  asm volatile("global_load_dwordx2 %0, %1, %2 offset:%3" : "=v"(d) : "v"(voff), "s"(sb), "i"(imm) : "memory")
#define GLD1(d, voff, sb, imm)  asm volatile("global_load_dword %0, %1, %2 offset:%3" : "=v"(d) : "v"(voff), "s"(sb), "i"(imm) : "memory")
#define GLD4S(d, voff, sb, imm) asm volatile("global_load_dwordx4 %0, %1, %2 offset:%3 sc1" : "=v"(d) : "v"(voff), "s"(sb), "i"(imm) : "memory")
#define GLD2S(d, voff, sb, imm) asm volatile("global_load_dwordx2 %0, %1, %2 offset:%3 sc1" : "=v"(d) : "v"(voff), "s"(sb), "i"(imm) : "memory")
#define GLD1S(d, voff, sb, imm) asm volatile("global_load_dword %0, %1, %2 offset:%3 sc1" : "=v"(d) : "v"(voff), "s"(sb), "i"(imm) : "memory")
#define WAIT_VM(n) asm volatile("s_waitcnt vmcnt(%0)" :: "i"(n) : "memory")
#define WAIT_VM1(n, a) asm volatile("s_waitcnt vmcnt(%1)" : "+v"(a) : "i"(n) : "memory")
#define WAIT_VM2(n, a, b) asm volatile("s_waitcnt vmcnt(%2)" : "+v"(a), "+v"(b) : "i"(n) : "memory")
#define WAIT_VM4(n, a, b, c, d) asm volatile("s_waitcnt vmcnt(%4)" : "+v"(a), "+v"(b), "+v"(c), "+v"(d) : "i"(n) : "memory")
#define WAIT_LGKM() asm volatile("s_waitcnt lgkmcnt(0)" ::: "memory")
#define MEMFENCE() asm volatile("" ::: "memory")
DI void bar() { asm volatile("s_waitcnt lgkmcnt(0)" ::: "memory"); __builtin_amdgcn_s_barrier(); asm volatile("" ::: "memory"); }
#define SCHED_FENCE() __builtin_amdgcn_sched_barrier(0)
#define RAW_BAR() do { asm volatile("" ::: "memory"); __builtin_amdgcn_s_barrier(); asm volatile("" ::: "memory"); } while (0)
#define ILVX(n, d, v) do { _Pragma("unroll") for (int z_ = 0; z_ < (n); ++z_) { __builtin_amdgcn_sched_group_barrier(0x008, 1, 0); if ((d) > 0) __builtin_amdgcn_sched_group_barrier(0x100, (d), 0); if ((v) > 0) __builtin_amdgcn_sched_group_barrier(0x002, (v), 0); } } while (0)

DI s16x8 tr_frag(LAS char* lds, unsigned a0, unsigned a1) {
  s16x4 a = __builtin_amdgcn_ds_read_tr16_b64_v4i16((LAS s16x4*)(lds + a0));
  s16x4 b = __builtin_amdgcn_ds_read_tr16_b64_v4i16((LAS s16x4*)(lds + a1));
  return __builtin_shufflevector(a, b, 0, 1, 2, 3, 4, 5, 6, 7);
}
DI f32x4 mfma32(s16x8 a, s16x8 b, f32x4 c) { return __builtin_amdgcn_mfma_f32_16x16x32_bf16(a, b, c, 0, 0, 0); }
DI f32x4 mfma16(s16x4 a, s16x4 b, f32x4 c) { return __builtin_amdgcn_mfma_f32_16x16x16bf16_1k(a, b, c, 0, 0, 0); }
DI f32x4 mfma4(float a, float b, f32x4 c) { return __builtin_amdgcn_mfma_f32_16x16x4f32(a, b, c, 0, 0, 0); }
DI float phi(float t) { return fmaxf(t, 0.f) + __builtin_amdgcn_fmed3f(__builtin_amdgcn_exp2f(t * 1.44269504089f), 0.f, 1.f) + 1e-6f; }
DI s16x8 as_frag(u32x4 v) { return __builtin_bit_cast(s16x8, v); }
DI u32x4 as_u4(s16x8 v) { return __builtin_bit_cast(u32x4, v); }
DI f32x4 as_f4(u32x4 v) { return __builtin_bit_cast(f32x4, v); }
DI float diag4(f32x4 d, int r) { const float t = (r & 1) ? d[1] : d[0], u = (r & 1) ? d[3] : d[2]; return (r & 2) ? u : t; }

typedef __amdgpu_buffer_rsrc_t rsrc_t;
DI void st16_sc1(rsrc_t rs, unsigned off, u32x4 v) { __builtin_amdgcn_raw_buffer_store_b128(v, rs, off, 0, 16); }
DI void st8_sc1(rsrc_t rs, unsigned off, u32x2 v) { __builtin_amdgcn_raw_buffer_store_b64(v, rs, off, 0, 16); }

DI void wait_flags(const char* ws, unsigned off, unsigned tlo, unsigned thi) {
#pragma nounroll
  for (int it = 0; it < (1 << 18); ++it) {
    u32x2 v;
    asm volatile("global_load_dwordx2 %0, %1, %2 sc1\n\ts_waitcnt vmcnt(0)" : "=v"(v) : "v"(off), "s"(ws) : "memory");
    const bool ok = (v.x == tlo) & (v.y == thi);
    if (__builtin_amdgcn_ballot_w64(ok) == ~0ull) break;
    __builtin_amdgcn_s_sleep(1);
  }
  asm volatile("" ::: "memory");
}

#define TS(i)

DI unsigned g64_rd(int g, int q, int pp, int u, int b) {
  int rho = 4 * g + q;
  return (unsigned)(128 * rho + 16 * ((2 * pp + u) ^ ((rho >> 1) & 3)) + 8 * (b ^ (pp & 1)));
}
DI f32x2 gelu2(f32x2 v) {
  const f32x2 t = v * v;
  const f32x2 a = v * (t * (f32x2){-0.1029432397f, -0.1029432397f} + (f32x2){-2.3022082f, -2.3022082f});
  const f32x2 d = (f32x2){__builtin_amdgcn_exp2f(a.x), __builtin_amdgcn_exp2f(a.y)} + (f32x2){1.f, 1.f};
  return v * (f32x2){__builtin_amdgcn_rcpf(d.x), __builtin_amdgcn_rcpf(d.y)};
}
DI int w2_row(int e, int n, int rho) {
  const int gg = (rho >> 2) & 3, j = 4 * (rho >> 4) + (rho & 3);
  if (n < 6) { const int cp = 2 * e + (n & 1), u = n >> 1, jj = j ^ (4 * (gg & 1)); return u < 2 ? 112 * cp + 16 * gg + 8 * u + jj : 112 * cp + 64 + 8 * gg + jj; }
  return 112 * (2 * e + (j >> 2)) + 96 + 4 * gg + (j & 3);
}

#define CODE_LINES 0
#define QG_ISSUE 3
#define QG_FLAG 6
#define P2ILV()

#define T4_HERE 1
#define USE_T1 1
__global__ __launch_bounds__(512, 2) void fused_block(const bf16_t* __restrict__ x, const bf16_t* __restrict__ wqkv, const bf16_t* __restrict__ wo,
                                                      const bf16_t* __restrict__ w1, const bf16_t* __restrict__ b1, const bf16_t* __restrict__ w2,
                                                      const bf16_t* __restrict__ b2, char* ws, bf16_t* __restrict__ out) {
  extern __shared__ __attribute__((aligned(16))) char smem_[];
  LAS char* lds = (LAS char*)smem_;
  asm volatile("" :: "s"(x), "s"(wqkv), "s"(wo), "s"(w1), "s"(b1), "s"(w2), "s"(b2), "s"(ws), "s"(out));
  const int tid = threadIdx.x, lane = tid & 63;
  const int w = __builtin_amdgcn_readfirstlane(tid >> 6);
  const int bid = blockIdx.x;
  const int P = (bid & 7) * 2 + (bid >> 7), C = (bid >> 3) & 15, J = bid >> 3;
  const int g = lane >> 4, r = lane & 15, q = (lane >> 2) & 3, pp = lane & 3;
  const unsigned ldsb = (unsigned)(size_t)smem_;
  const rsrc_t rs = __builtin_amdgcn_make_buffer_rsrc(ws, 0, 0x7fffffff, 0x00020000);
  const unsigned l16 = (unsigned)lane * 16u;
  TS(0);
  const f32x4 zero4 = (f32x4){0, 0, 0, 0};
  const int ct = C & 7, th = C >> 3;
  constexpr unsigned L_WKV = 0, L_X = 65536, L_CS = 16384, L_T = 53248, L_G32 = 65536, L_SP = 114688;
  s16x8 ones; for (int j = 0; j < 8; ++j) ones[j] = (short)0x3f80;

  auto issue_w1p = [&](int kb, int pc) {
    auto srow = [&](int rho) { return 64 * (kb >> 1) + 16 * ((rho >> 2) & 3) + 8 * (kb & 1) + 4 * (rho >> 4) + (rho & 3); };
    if (pc < 4) { int rho = pc * 8 + (lane >> 3); glds16(w1, (unsigned)(srow(rho) * 3584 + C * 224 + (((lane & 7) ^ ((rho >> 1) & 3)) * 16)), ldsb + kb * 4096 + pc * 1024); }
    else if (pc < 6) { int rho = (pc - 4) * 16 + (lane >> 2); glds16(w1, (unsigned)(srow(rho) * 3584 + C * 224 + 128 + (((lane & 3) ^ ((rho >> 2) & 1)) * 16)), ldsb + L_G32 + kb * 3072 + (pc - 4) * 1024); }
    else { int rho = lane >> 1; glds16(w1, (unsigned)(srow(rho) * 3584 + C * 224 + 192 + (lane & 1) * 16), ldsb + L_G32 + kb * 3072 + 2048); }
  };
  unsigned rd4[4];
#pragma unroll
  for (int t = 0; t < 4; ++t) rd4[t] = g64_rd(g, q, pp, t >> 1, t & 1);
  auto lin64 = [&](int kb) {
    s16x8 f[4];
#pragma unroll
    for (int i = 0; i < 4; ++i) { const unsigned a = (unsigned)(kb * 4096) + rd4[i]; f[i] = tr_frag(lds, a, a + 2048); }
    WAIT_LGKM();
#pragma unroll
    for (int i = 0; i < 4; ++i) *(LAS s16x8*)(lds + kb * 4096 + i * 1024 + l16) = f[i];
  };
  const int m2 = w >> 2, kq = w & 3;

  u32x4 pq;
  unsigned tlo, thi;
  u32x4 xr = (u32x4){0, 0, 0, 0};
  const unsigned xs = L_X + (unsigned)(w * 12288);
  {
    u32x2 Ev;
    u32x4 xf[16];
    const int xrow = lane >> 3;
    const unsigned xoff = (unsigned)(((P * 128 + w * 16 + xrow) * 512) * 2 + (((lane & 7) ^ (xrow & 7)) * 16));
    const int wk = lane >> 2;
    const unsigned wcol = (unsigned)((((((lane >> 1) & 1) ^ ((wk >> 3) & 1)) * 16) + (lane & 1) * 8) * 2);
    const unsigned woff = (unsigned)(wk * 1536 * 2) + wcol + (unsigned)(C * 64);
    auto issue_x = [&](int ch) {
      const unsigned dst = ldsb + xs + (ch % 6) * 2048;
      glds16(x, xoff + ch * 128, dst);
      glds16(x, xoff + ch * 128 + 8 * 1024, dst + 1024);
    };
    auto issue_wkv = [&](int ch) {
      int sub = w >> 1, seg = w & 1;
      glds16(wqkv, woff + (unsigned)((ch * 64 + sub * 16) * 3072 + (1 + seg) * 1024), ldsb + L_WKV + ch * 8192 + w * 1024);
    };
    auto issue_wq = [&](int i) {
      int blk = w * 4 + i;
      glds16(wqkv, woff + (unsigned)(blk * 16 * 3072), ldsb + xs + 4096 + i * 1024);
    };
    const int worow = lane >> 3;
    const unsigned wooff = (unsigned)(worow * 1024 + ct * 128 + (((lane & 7) ^ ((worow >> 1) & 3)) * 16));
    auto issue_wo = [&](int i) { int blk = w * 8 + i; glds16(wo, wooff + blk * 8192, ldsb + xs + (i < 4 ? 8192 + i * 1024 : (i - 4) * 1024)); };
    unsigned tc0 = 0, tc1 = 0;
#pragma unroll
    for (int ch = 0; ch < 8; ++ch) {
      issue_wkv(ch);
      if (ch < 6) issue_x(ch);
      if (ch == 1) {
        RAW_BAR();
        if (w < 6 && USE_T1) {
          if (lane < 32) GLD1(tc0, (unsigned)((w + 2) * 64 + 2 * J + g) * 3072u + 1024u + (unsigned)r * 128u, wqkv, 0);
          else if (lane < 40) GLD1(tc1, (unsigned)((bid & 7) * 256 + J * 8 + (lane - 32)) * 1024u + (unsigned)(w + 2) * 128u, x, 0);
        } else if (lane < 8) {
          const unsigned cl = (unsigned)((J * 2 + (w & 1)) * 16 + lane) & 255u;
          const char* cb = CODE_LINES > 0 ? (const char*)__builtin_amdgcn_s_getpc() : (const char*)x;
          GLD1(tc0, (cl < CODE_LINES ? cl : 0u) * 128u, cb, 0);
          GLD1(tc1, (cl + 8u < CODE_LINES ? cl + 8u : 0u) * 128u, cb, 0);
        }
      }
      if (ch == 3) RAW_BAR();
    }
    asm volatile("global_load_dwordx2 %0, %1, %2 sc1" : "=v"(Ev) : "v"(WS_E), "s"(ws) : "memory");
    TS(53);
    const unsigned wrd = (unsigned)((g >> 1) * 1024 + (8 * (g & 1) + q) * 64 + 8 * pp);
    const unsigned wrd0 = L_WKV + (g >> 1) * 1024 + wrd + 32u * (0 ^ (g & 1)), wrd1 = L_WKV + (g >> 1) * 1024 + wrd + 32u * (1 ^ (g & 1));
    const unsigned wrdc = ((w & 2) ? wrd1 : wrd0) + (unsigned)((w >> 2) * 4096 + (w & 1) * 1024);
    const unsigned xrd0 = xs + r * 128 + (unsigned)(((0 + g) ^ (r & 7)) * 16), xrd1 = xs + r * 128 + (unsigned)(((4 + g) ^ (r & 7)) * 16);
    const unsigned xrr = L_X + (unsigned)((4 * th + (w >> 1)) * 12288 + (ct % 6) * 2048 + r * 128 + (((2 * g + (w & 1)) ^ (r & 7)) * 16));

    s16x8 wf[2][2][4];
    s16x8 cf, cf7;
    f32x4 acc[4], cl = zero4, sa = zero4, qa = zero4;
    unsigned tw0 = 0, tw1 = 0, tw2 = 0;
    acc[0] = acc[1] = acc[2] = acc[3] = zero4;
#pragma unroll
    for (int ch = 0; ch <= 8; ++ch) {
      if (ch < 8) {
        if (ch == 2) WAIT_VM2(14, tc0, tc1);
        else WAIT_VM(ch == 0 ? 20 : ch == 1 ? 17 : ch == 3 ? 13 : ch == 4 ? 12 : ch == 5 ? 11 : 8);
        xf[2 * ch] = *(LAS u32x4*)(lds + xrd0 + (ch % 6) * 2048);
        xf[2 * ch + 1] = *(LAS u32x4*)(lds + xrd1 + (ch % 6) * 2048);
        bar();
        if (ch == 0) TS(45); if (ch == 2) TS(47); if (ch == 4) TS(49); if (ch == 6) TS(51); if (ch == 7) TS(52);
        if (ch == 1 || ch == 2) issue_x(5 + ch);
        else if (ch == 3 || ch == 4) { issue_wq(2 * (ch - 3)); issue_wq(2 * (ch - 3) + 1); }
        else if (ch >= 5) { issue_wo(2 * (ch - 5)); issue_wo(2 * (ch - 5) + 1); }
        if (ch == ct) xr = *(LAS u32x4*)(lds + xrr);
        SCHED_FENCE();
#pragma unroll
        for (int s = 0; s < 2; ++s)
#pragma unroll
          for (int t = 0; t < 4; ++t) {
            const unsigned a = ((t >> 1) ? wrd1 : wrd0) + ch * 8192 + s * 4096 + (t & 1) * 1024;
            wf[ch & 1][s][t] = tr_frag(lds, a, a + 256);
          }
      }
      if (ch > 0) {
        const int c0 = ch - 1;
        if (c0 < 6) cl = mfma32(ones, cf, cl);
#pragma unroll
        for (int s = 0; s < 2; ++s) {
          const s16x8 xa = as_frag(xf[c0 * 2 + s]);
#pragma unroll
          for (int t = 0; t < 4; ++t) acc[t] = mfma32(xa, wf[c0 & 1][s][t], acc[t]);
          sa = mfma32(ones, xa, sa);
          qa = mfma32(xa, xa, qa);
        }
        if (ch < 8) ILVX(8, 2, 0);
      }
      if (ch < 7) cf = tr_frag(lds, wrdc + ch * 8192, wrdc + ch * 8192 + 256);
      if (ch == 6) {
        cf7 = tr_frag(lds, wrdc + 7 * 8192, wrdc + 7 * 8192 + 256);
        cl = mfma32(ones, cf, cl);
        cl = mfma32(ones, cf7, cl);
        if (g == 0) *(LAS float*)(lds + L_CS + r * 32 + w * 4) = cl[0];
      }
      SCHED_FENCE();
    }
    TS(1);
    WAIT_VM1(6, Ev);
    if (w >= 2 && (T4_HERE || w < 5)) {
      const char* tb = (w < 5 ? (const char*)w1 : (const char*)w2) + J * 57344;
      const unsigned u = (unsigned)((w < 5 ? w - 2 : w - 5) * 150 + (lane < 50 ? lane : 49));
      GLD1(tw0, u * 128u, tb, 0); GLD1(tw1, (u + 50u) * 128u, tb, 0); GLD1(tw2, (u + 100u < 448u ? u + 100u : 447u) * 128u, tb, 0);
    }
    const unsigned a0 = L_X + 4096 + wrd + 32u * (0 ^ (g & 1)), a1 = L_X + 4096 + wrd + 32u * (1 ^ (g & 1));
    float mean1, rstd1;
    {
      const float qd = (g == (r >> 2)) ? diag4(qa, r) : 0.f;
      const f32x4 Sr = mfma4(sa[0], 0.25f, zero4), Qr = mfma4(qd, 1.f, zero4);
      const f32x4 cs = *(LAS f32x4*)(lds + L_CS + r * 32) + *(LAS f32x4*)(lds + L_CS + r * 32 + 16);
      const float Qc = mfma4(1.f, qd, zero4)[0];
      float val[4][4];
#pragma unroll
      for (int j = 0; j < 4; ++j) {
        const float mu = Sr[j] * (1.f / 512.f); const float var = fmaxf(Qr[j] * (1.f / 512.f) - mu * mu, 0.f); const float rsd = __builtin_amdgcn_rsqf(var + 1e-5f);
#pragma unroll
        for (int t = 0; t < 4; ++t) { const float v = rsd * (acc[t][j] - mu * cs[t]); val[t][j] = (t & 1) ? v : phi(v); }
      }
      s16x4 one4; for (int j = 0; j < 4; ++j) one4[j] = (short)0x3f80;
#pragma unroll
      for (int hd = 0; hd < 2; ++hd) {
        const s16x4 pk4 = __builtin_bit_cast(s16x4, (u32x2){pk_bf16(val[2 * hd][0], val[2 * hd][1]), pk_bf16(val[2 * hd][2], val[2 * hd][3])});
        const s16x4 v4 = __builtin_bit_cast(s16x4, (u32x2){pk_bf16(val[2 * hd + 1][0], val[2 * hd + 1][1]), pk_bf16(val[2 * hd + 1][2], val[2 * hd + 1][3])});
        const f32x4 kvp = mfma16(pk4, v4, zero4), zp = mfma16(pk4, one4, zero4);
        *(LAS f32x4*)(lds + hd * 8192 + w * 1024 + l16) = kvp;
        if (r == 0) *(LAS f32x4*)(lds + L_T + 1024 + (hd * 8 + w) * 64 + g * 16) = zp;
      }
      mean1 = sa[0] * (1.f / 512.f); rstd1 = __builtin_amdgcn_rsqf(fmaxf(Qc * (1.f / 512.f) - mean1 * mean1, 0.f) + 1e-5f);
    }
    SCHED_FENCE();
    TS(22);
    {
      const unsigned o = (unsigned)(w * 12288);
      f32x4 c0 = mfma32(tr_frag(lds, a0 + o, a0 + o + 256), ones, zero4), c1 = mfma32(tr_frag(lds, a1 + o, a1 + o + 256), ones, zero4);
      c0 = mfma32(tr_frag(lds, a0 + o + 2048, a0 + o + 2304), ones, c0); c1 = mfma32(tr_frag(lds, a1 + o + 2048, a1 + o + 2304), ones, c1);
      if (r == 0) { *(LAS f32x4*)(lds + L_T + w * 128 + g * 32) = c0; *(LAS f32x4*)(lds + L_T + w * 128 + g * 32 + 16) = c1; }
    }
    bar();
    TS(24);
    {
      const u64 E = ((u64)(unsigned)__builtin_amdgcn_readfirstlane(Ev.y) << 32) | (unsigned)__builtin_amdgcn_readfirstlane(Ev.x);
      const u64 T = E * 0x9E3779B97F4A7C15ull + 0xD1B54A32D192ED03ull;
      tlo = (unsigned)T; thi = (unsigned)(T >> 32);
    }
    if (w < 2) {
      f32x4 s = zero4, sz = zero4;
#pragma unroll
      for (int mm = 0; mm < 8; ++mm) {
        s += *(LAS f32x4*)(lds + w * 8192 + mm * 1024 + l16);
        sz += *(LAS f32x4*)(lds + L_T + 1024 + (w * 8 + mm) * 64 + (lane & 3) * 16);
      }
      const unsigned dst = WS_KVP + (unsigned)(((C * 2 + w) * 16 + P) * KVP_STRIDE) + l16;
      st16_sc1(rs, dst, __builtin_bit_cast(u32x4, s));
      if (lane < 4) st16_sc1(rs, dst + 1024, __builtin_bit_cast(u32x4, sz));
    }
    SCHED_FENCE();
    f32x4 aq[2];
    aq[0] = aq[1] = zero4;
    {
      s16x8 f[2][4];
#pragma unroll
      for (int kk = 0; kk <= 8; ++kk) {
        if (kk < 8) {
#pragma unroll
          for (int s = 0; s < 2; ++s) {
            f[kk & 1][s * 2 + 0] = tr_frag(lds, a0 + kk * 12288 + s * 2048, a0 + kk * 12288 + s * 2048 + 256);
            f[kk & 1][s * 2 + 1] = tr_frag(lds, a1 + kk * 12288 + s * 2048, a1 + kk * 12288 + s * 2048 + 256);
          }
        }
        if (kk > 0) {
          const int k0 = kk - 1;
#pragma unroll
          for (int s = 0; s < 2; ++s) {
            aq[0] = mfma32(f[k0 & 1][s * 2 + 0], as_frag(xf[k0 * 2 + s]), aq[0]);
            aq[1] = mfma32(f[k0 & 1][s * 2 + 1], as_frag(xf[k0 * 2 + s]), aq[1]);
          }
          if (kk < 8) ILVX(4, 2, 0);
        }
        SCHED_FENCE();
        if (kk == QG_ISSUE) {
          issue_wo(6); issue_wo(7);
#pragma unroll
          for (int i = 0; i < 4; ++i) issue_w1p(2 * w, i);
          if (w != 6) {
#pragma unroll
            for (int i = 0; i < 4; ++i) issue_w1p(2 * w + 1, i);
          }
          SCHED_FENCE();
        }
        if (kk == QG_FLAG) {
          if (w < 2) {
            WAIT_VM(10);
            if (lane == 0) st8_sc1(rs, WS_F1 + (unsigned)(((C * 2 + w) * 16 + P) * 128), (u32x2){tlo, thi});
          }
          TS(2);
          SCHED_FENCE();
        }
      }
    }
    TS(25);
    {
      f32x4 c0 = zero4, c1 = zero4;
#pragma unroll
      for (int ww = 0; ww < 8; ++ww) { c0 += *(LAS f32x4*)(lds + L_T + ww * 128 + g * 32); c1 += *(LAS f32x4*)(lds + L_T + ww * 128 + g * 32 + 16); }
      float a[4], b[4];
#pragma unroll
      for (int j = 0; j < 4; ++j) { a[j] = phi(rstd1 * (aq[0][j] - mean1 * c0[j])); b[j] = phi(rstd1 * (aq[1][j] - mean1 * c1[j])); }
      pq.x = pk_bf16(a[0], a[1]); pq.y = pk_bf16(a[2], a[3]); pq.z = pk_bf16(b[0], b[1]); pq.w = pk_bf16(b[2], b[3]);
    }
    asm volatile("" : "+v"(pq), "+v"(xr));
    WAIT_VM4(0, tw0, tw1, tw2, pq);
  }
  TS(4);
  if ((w >> 1) == 1) {
    const int hd = w & 1;
    wait_flags(ws, WS_F1 + (unsigned)(((C * 2 + hd) * 16 + r) * 128), tlo, thi);
    TS(5);
    const char* kb0 = ws + WS_KVP + (unsigned)((C * 2 + hd) * 16 * KVP_STRIDE);
    u32x4 a[16];
    unsigned zz[4];
#pragma unroll
    for (int i = 0; i < 16; ++i) GLD4S(a[i], l16, kb0 + (i >> 1) * 2 * KVP_STRIDE, (i & 1) * KVP_STRIDE);
    const unsigned zoff = (unsigned)(g * KVP_STRIDE + 1024 + r * 4);
#pragma unroll
    for (int i = 0; i < 4; ++i) GLD1S(zz[i], zoff, kb0 + i * 4 * KVP_STRIDE, 0);
    f32x4 s = zero4;
#pragma unroll
    for (int i = 0; i < 16; i += 4) {
      WAIT_VM4(16 - i, a[i], a[i + 1], a[i + 2], a[i + 3]);
      s += (as_f4(a[i]) + as_f4(a[i + 1])) + (as_f4(a[i + 2]) + as_f4(a[i + 3]));
    }
    *(LAS u32x2*)(lds + L_T + 2048 + hd * 1024 + lane * 8) = (u32x2){pk_bf16(s[0], s[1]), pk_bf16(s[2], s[3])};
    WAIT_VM4(0, zz[0], zz[1], zz[2], zz[3]);
    const f32x4 z = mfma4((__uint_as_float(zz[0]) + __uint_as_float(zz[1])) + (__uint_as_float(zz[2]) + __uint_as_float(zz[3])), 1.f, zero4);
    *(LAS u32x2*)(lds + L_T + 2048 + hd * 1024 + 512 + lane * 8) = (u32x2){pk_bf16(z[0], z[1]), pk_bf16(z[2], z[3])};
  } else {
    lin64(2 * w);
    if (w != 6) lin64(2 * w + 1);
  }
  TS(29);
  bar();
  {
    u32x4 o;
#pragma unroll
    for (int hd = 0; hd < 2; ++hd) {
      const s16x4 kvA = *(LAS s16x4*)(lds + L_T + 2048 + hd * 1024 + lane * 8);
      const s16x4 zA = *(LAS s16x4*)(lds + L_T + 2048 + hd * 1024 + 512 + lane * 8);
      u32x2 pu; pu.x = hd ? pq.z : pq.x; pu.y = hd ? pq.w : pq.y;
      const s16x4 p4 = __builtin_bit_cast(s16x4, pu);
      const f32x4 num = mfma16(kvA, p4, zero4);
      const f32x4 den = mfma16(zA, p4, zero4);
      const float inv = __builtin_amdgcn_rcpf(den[0] + 1e-6f);
      if (hd == 0) { o.x = pk_bf16(num[0] * inv, num[1] * inv); o.y = pk_bf16(num[2] * inv, num[3] * inv); }
      else { o.z = pk_bf16(num[0] * inv, num[1] * inv); o.w = pk_bf16(num[2] * inv, num[3] * inv); }
    }
    st16_sc1(rs, WS_O + (unsigned)(((P * 8 + w) * 16 + C) * 1024) + l16, o);
  }
  SCHED_FENCE();
  TS(30);
  s16x8 wof[4][4];
#pragma unroll
  for (int i = 0; i < 4; ++i)
#pragma unroll
    for (int t = 0; t < 4; ++t) { const unsigned a = L_X + (unsigned)((kq * 2 + (i >> 1)) * 12288 + ((i & 1) ? 0 : 8192)) + rd4[t]; wof[i][t] = tr_frag(lds, a, a + 2048); }
  SCHED_FENCE();
  WAIT_VM(0);
  if (lane == 0) st8_sc1(rs, WS_F2 + (unsigned)(((P * 8 + w) * 16 + C) * 128), (u32x2){tlo, thi});
  TS(6);
  bar();
  const int tile0 = P * 8 + th * 4 + 2 * m2;
  const int tilef = tile0 + (kq >> 1), khf = kq & 1;
  u32x4 bb2;
  u32x2 bb1;
  if ((w >> 1) == 1) { lin64(2 * w); lin64(2 * w + 1); }
  if (w == 6) {
#pragma unroll
    for (int i = 0; i < 4; ++i) issue_w1p(13, i);
  }
#pragma unroll
  for (int i = 0; i < 6; ++i) issue_w1p(2 * w + (i / 3), 4 + i % 3);
  {
    const unsigned b2o = (unsigned)((ct * 64 + 16 * g + 8 * khf) * 2);
    GLD4(bb2, b2o, b2, 0);
    const int wt = w < 7 ? w : 6;
    const int col = wt < 4 ? 16 * g + 8 * (wt >> 1) + 4 * ((wt & 1) ^ (g & 1)) : wt < 6 ? 64 + 8 * g + 4 * ((wt & 1) ^ (g & 1)) : 96 + 4 * g;
    const unsigned b1o = (unsigned)((C * 112 + col) * 2);
    GLD2(bb1, b1o, b1, 0);
  }
  TS(28);
  wait_flags(ws, WS_F2 + (unsigned)(((tile0 + ((lane >> 2) & 1)) * 16 + 4 * kq + (lane & 3)) * 128), tlo, thi);
  TS(7);
  asm volatile("" : "+v"(bb2), "+v"(bb1));
  u32x4 hb;
  {
    u32x4 of[2][4];
    const char* ob = ws + WS_O + (unsigned)((tile0 * 16 + kq * 4) * 1024);
#pragma unroll
    for (int i = 0; i < 4; ++i)
#pragma unroll
      for (int tt = 0; tt < 2; ++tt) GLD4S(of[tt][i], l16, ob + tt * 16384, i * 1024);
    f32x4 acc[2][4];
#pragma unroll
    for (int t = 0; t < 4; ++t) acc[0][t] = acc[1][t] = zero4;
#pragma unroll
    for (int i = 0; i < 4; ++i) {
      WAIT_VM2(6 - 2 * i, of[0][i], of[1][i]);
#pragma unroll
      for (int tt = 0; tt < 2; ++tt)
#pragma unroll
        for (int t = 0; t < 4; ++t) acc[tt][t] = mfma32(wof[i][t], as_frag(of[tt][i]), acc[tt][t]);
      SCHED_FENCE();
    }
    f32x4 k0 = zero4, k1 = zero4;
#pragma unroll
    for (int d = 0; d < 4; ++d) {
      if (d == kq) { k0 = acc[d >> 1][2 * (d & 1)]; k1 = acc[d >> 1][2 * (d & 1) + 1]; }
      else {
        LAS char* dst = lds + L_SP + ((m2 * 4 + d) * 3 + ((kq - d - 1) & 3)) * 2048 + l16;
        *(LAS f32x4*)dst = acc[d >> 1][2 * (d & 1)];
        *(LAS f32x4*)(dst + 1024) = acc[d >> 1][2 * (d & 1) + 1];
      }
    }
    TS(32);
    bar();
    TS(33);
#pragma unroll
    for (int s = 0; s < 3; ++s) {
      k0 += *(LAS f32x4*)(lds + L_SP + (w * 3 + s) * 2048 + l16);
      k1 += *(LAS f32x4*)(lds + L_SP + (w * 3 + s) * 2048 + 1024 + l16);
    }
    const f32x4 A = (g & 1) ? k1 : k0, B = (g & 1) ? k0 : k1;
    hb.x = pk_bf16(lo_f(xr.x) + A[0], hi_f(xr.x) + A[1]); hb.y = pk_bf16(lo_f(xr.y) + A[2], hi_f(xr.y) + A[3]);
    hb.z = pk_bf16(lo_f(xr.z) + B[0], hi_f(xr.z) + B[1]); hb.w = pk_bf16(lo_f(xr.w) + B[2], hi_f(xr.w) + B[3]);
    const unsigned blk = (unsigned)(tilef * 16 + 2 * ct + khf);
    st16_sc1(rs, WS_H + blk * 1024 + l16, hb);
    const f32x4 sS = mfma32(ones, as_frag(hb), zero4), sQ = mfma32(as_frag(hb), as_frag(hb), zero4);
    if (g == (r >> 2)) st8_sc1(rs, WS_HS + blk * 128 + (unsigned)r * 8u, (u32x2){__float_as_uint(sS[0]), __float_as_uint(diag4(sQ, r))});
  }
  TS(34);
  WAIT_VM(0);
  if (lane == 0) {
    st8_sc1(rs, WS_F3 + (unsigned)((tilef * 16 + 2 * ct + khf) * 128), (u32x2){tlo, thi});
    if (bid == 0 && w == 0) st8_sc1(rs, WS_E, (u32x2){tlo, thi});
  }
  TS(8);
  const unsigned slot = L_SP + (unsigned)(w * 6144);
  constexpr unsigned L_CNT = L_SP + 7 * 6144 + 4096;
  auto issue_w2 = [&](int n, unsigned dst) {
#pragma unroll
    for (int b = 0; b < 4; ++b) {
      const int rho = b * 8 + (lane >> 3);
      glds16(w2, (unsigned)(w2_row(w, n, rho) * 1024 + ct * 128 + (((lane & 7) ^ ((rho >> 1) & 3)) * 16)), ldsb + dst + b * 1024);
    }
  };
  s16x8 wr[4][4];
  auto read_w2 = [&](s16x8* f, unsigned src) {
#pragma unroll
    for (int t = 0; t < 4; ++t) f[t] = tr_frag(lds, src + rd4[t], src + rd4[t] + 2048);
  };
  auto cnt_add = [&](unsigned a) { if (lane == 0) __hip_atomic_fetch_add((LAS unsigned*)(lds + a), 1u, __ATOMIC_RELAXED, __HIP_MEMORY_SCOPE_WORKGROUP); MEMFENCE(); };
  auto cnt_wait = [&](unsigned a) {
#pragma nounroll
    for (int it = 0; it < (1 << 20); ++it) {
      MEMFENCE();
      const unsigned v = *(volatile LAS unsigned*)(lds + a);
      if (__builtin_amdgcn_readfirstlane(v) >= 8u) break;
      __builtin_amdgcn_s_sleep(1);
    }
    MEMFENCE();
  };
  issue_w2(0, slot);
  {
    const int rho0 = 4 * g + q;
    s16x8 f[6];
#pragma unroll
    for (int i = 0; i < 2; ++i) {
      const unsigned base = L_G32 + (unsigned)((2 * w + i) * 3072);
#pragma unroll
      for (int b = 0; b < 2; ++b) { const unsigned a = base + (unsigned)(64 * rho0 + 16 * (pp ^ ((rho0 >> 2) & 1)) + 8 * (b ^ (pp & 1))); f[i * 3 + b] = tr_frag(lds, a, a + 1024); }
      const unsigned a = base + 2048u + (unsigned)(32 * rho0 + 8 * pp);
      f[i * 3 + 2] = tr_frag(lds, a, a + 512);
    }
    WAIT_LGKM();
#pragma unroll
    for (int i = 0; i < 6; ++i) *(LAS s16x8*)(lds + L_G32 + w * 6144 + i * 1024 + l16) = f[i];
  }
  if (w == 6) lin64(13);
  bar();
  TS(35);
  if (w < 7) {
    const unsigned a0 = (w < 4 ? (unsigned)(w * 1024) : L_G32 + (unsigned)((w - 4) * 1024)) + l16, st = w < 4 ? 4096u : 3072u;
    f32x4 c0 = zero4, c1 = zero4;
#pragma unroll
    for (int i = 0; i < 16; i += 2) {
      c0 = mfma32(*(LAS s16x8*)(lds + a0 + i * st), ones, c0);
      c1 = mfma32(*(LAS s16x8*)(lds + a0 + (i + 1) * st), ones, c1);
    }
    if (r == 0) {
      *(LAS f32x4*)(lds + slot + 4096 + g * 32) = c0 + c1;
      *(LAS f32x4*)(lds + slot + 4096 + g * 32 + 16) = (f32x4){lo_f(bb1.x), hi_f(bb1.x), lo_f(bb1.y), hi_f(bb1.y)};
    }
  } else if (lane == 0) *(LAS u32x2*)(lds + L_CNT) = (u32x2){0u, 0u};
  WAIT_VM(0);
  read_w2(wr[0], slot);
  bar();
  issue_w2(1, slot);
  TS(36);
  const int tile3 = P * 8 + w;
  wait_flags(ws, WS_F3 + (unsigned)((tile3 * 16 + r) * 128), tlo, thi);
  TS(9);
  {
    u32x4 hf[16];
    u32x2 sv[4];
    const char* hbp = ws + WS_H + (unsigned)(tile3 * 16384);
    {
      const char* sp = ws + WS_HS + (unsigned)(tile3 * 2048);
      const unsigned l8 = (unsigned)lane * 8u;
#pragma unroll
      for (int i = 0; i < 4; ++i) GLD2S(sv[i], l8, sp, i * 512);
    }
#pragma unroll
    for (int i = 0; i < 16; ++i) GLD4S(hf[i], l16, hbp + (i >> 2) * 4096, (i & 3) * 1024);
    read_w2(wr[1], slot);
    if (w < 4) __builtin_amdgcn_s_setprio(3); else __builtin_amdgcn_s_setprio(2);
    f32x4 acc[7];
#pragma unroll
    for (int t = 0; t < 7; ++t) acc[t] = zero4;
    {
      s16x8 f[2][4];
#pragma unroll
      for (int i = 0; i <= 16; ++i) {
        if (i < 16) {
#pragma unroll
          for (int t = 0; t < 4; ++t) f[i & 1][t] = *(LAS s16x8*)(lds + i * 4096 + t * 1024 + l16);
        }
        if (i > 0) {
          const int i0 = i - 1;
#pragma unroll
          for (int t = 0; t < 4; ++t) acc[t] = mfma32(f[i0 & 1][t], as_frag(hf[i0]), acc[t]);
          if (i < 16) ILVX(4, 1, 0);
        }
        SCHED_FENCE();
        if (i < 16) WAIT_VM1(15 - i, hf[i]);
      }
    }
    TS(37);
    cnt_add(L_CNT);
    issue_w2(2, slot);
    asm volatile("" : "+v"(sv[0]), "+v"(sv[1]), "+v"(sv[2]), "+v"(sv[3]));
    float rr, mrr;
    {
      const float sp = (__uint_as_float(sv[0].x) + __uint_as_float(sv[1].x)) + (__uint_as_float(sv[2].x) + __uint_as_float(sv[3].x));
      const float qp = (__uint_as_float(sv[0].y) + __uint_as_float(sv[1].y)) + (__uint_as_float(sv[2].y) + __uint_as_float(sv[3].y));
      const float S = mfma4(1.f, sp, zero4)[0], Q = mfma4(1.f, qp, zero4)[0];
      const float mean = S * (1.f / 512.f);
      const float var = fmaxf(Q * (1.f / 512.f) - mean * mean, 0.f);
      const float r1 = __builtin_amdgcn_rsqf(var + 1e-5f);
      rr = r1 * __builtin_amdgcn_rsqf(var * r1 * r1 + 1e-5f);
      mrr = -mean * rr;
    }
    if (w < 4) __builtin_amdgcn_s_setprio(1);
    TS(38);
    auto epi = [&](int t) -> u32x2 {
      const f32x4 cs = *(LAS f32x4*)(lds + L_SP + t * 6144 + 4096 + g * 32), bv = *(LAS f32x4*)(lds + L_SP + t * 6144 + 4096 + g * 32 + 16);
      const f32x4 v = acc[t] * rr + (cs * mrr + bv);
      const f32x2 a = gelu2((f32x2){v[0], v[1]}), b = gelu2((f32x2){v[2], v[3]});
      return (u32x2){pk_bf16(a.x, a.y), pk_bf16(b.x, b.y)};
    };
    const unsigned dst = WS_HH + (unsigned)((tile3 * 8 + (C >> 1)) * 7168);
    const unsigned dstc = dst + (unsigned)((C & 1) * 3072) + l16;
    const unsigned foff = (unsigned)((tile3 * 16 + C) * 128);
    auto pass2 = [&](int i0) {
      s16x8 f[12];
#pragma unroll
      for (int i = 0; i < 12; ++i) f[i] = *(LAS s16x8*)(lds + L_G32 + i0 * 3072 + i * 1024 + l16);
#pragma unroll
      for (int i = 0; i < 4; ++i)
#pragma unroll
        for (int t = 0; t < 3; ++t) acc[4 + t] = mfma32(f[3 * i + t], as_frag(hf[i0 + i]), acc[4 + t]);
    };
    {
      pass2(0);
      const u32x2 e0 = epi(0);
      P2ILV();
      SCHED_FENCE();
      pass2(4);
      const u32x2 e1 = epi(1);
      P2ILV();
      st16_sc1(rs, dstc, (u32x4){e0.x, e0.y, e1.x, e1.y});
      SCHED_FENCE();
    }
    TS(39);
    {
      pass2(8);
      const u32x2 e2 = epi(2);
      P2ILV();
      SCHED_FENCE();
      pass2(12);
      const u32x2 e3 = epi(3);
      P2ILV();
      st16_sc1(rs, dstc + 1024, (u32x4){e2.x, e2.y, e3.x, e3.y});
      SCHED_FENCE();
    }
    cnt_add(L_CNT + 4);
    const u32x2 e4 = epi(4);
    SCHED_FENCE();
    WAIT_VM(0);
    if (lane == 0) st8_sc1(rs, WS_F4A + foff, (u32x2){tlo, thi});
    TS(10);
    read_w2(wr[2], slot);
    {
      const u32x2 e5 = epi(5);
      st16_sc1(rs, dstc + 2048, (u32x4){e4.x, e4.y, e5.x, e5.y});
      const u32x2 e6 = epi(6);
      st8_sc1(rs, dst + 6144 + l16 + (unsigned)((C & 1) * 8), e6);
    }
    WAIT_LGKM();
    issue_w2(3, slot);
    TS(40);
    WAIT_VM(4);
    if (lane == 0) st8_sc1(rs, WS_F4B + foff, (u32x2){tlo, thi});
    TS(11);
    __builtin_amdgcn_s_setprio(0);
    cnt_wait(L_CNT);
    issue_w2(4, (unsigned)(w * 8192));
    issue_w2(5, (unsigned)(w * 8192 + 4096));
    WAIT_VM(9);
    read_w2(wr[3], slot);
    WAIT_LGKM();
    issue_w2(6, slot);
  }
  TS(41);
  {
    const int tile4 = P * 8 + th * 4;
    const unsigned foff = (unsigned)(((tile4 + ((lane >> 1) & 3)) * 16 + 2 * w + (lane & 1)) * 128);
    const char* ab = ws + WS_HH + (unsigned)((tile4 * 8 + w) * 7168) + 4096;
    u32x4 af[7][4];
#define LD_A(n) do { _Pragma("unroll") for (int tt = 0; tt < 4; ++tt) GLD4S(af[n][tt], l16, ab + tt * 57344, ((n) < 6 ? ((n) & 1) * 3072 + ((n) >> 1) * 1024 : 6144) - 4096); } while (0)
#define MM_A(n, cnt) do { _Pragma("unroll") for (int h2 = 0; h2 < 2; ++h2) { WAIT_VM2((cnt) + 2 - 2 * h2, af[n][2 * h2], af[n][2 * h2 + 1]); \
      _Pragma("unroll") for (int tt = 2 * h2; tt < 2 * h2 + 2; ++tt) _Pragma("unroll") for (int t = 0; t < 4; ++t) acc[tt][t] = mfma32(wr[(n) & 3][t], as_frag(af[n][tt]), (n) == 0 ? zero4 : acc[tt][t]); \
      SCHED_FENCE(); } } while (0)
    wait_flags(ws, WS_F4A + foff, tlo, thi);
    TS(12);
    u32x2 fb;
    f32x4 acc[4][4];
    LD_A(0); LD_A(1);
    GLD2S(fb, foff, ws + WS_F4B, 0);
    LD_A(2); LD_A(3);
    MM_A(0, 13);
    MM_A(1, 9);
    WAIT_VM1(8, fb);
    if (__builtin_amdgcn_ballot_w64((fb.x == tlo) & (fb.y == thi)) != ~0ull) wait_flags(ws, WS_F4B + foff, tlo, thi);
    TS(42);
    LD_A(4); LD_A(5);
    read_w2(wr[0], (unsigned)(w * 8192));
    read_w2(wr[1], (unsigned)(w * 8192 + 4096));
    MM_A(2, 12);
    LD_A(6);
    read_w2(wr[2], slot);
    MM_A(3, 12);
    MM_A(4, 8);
    MM_A(5, 4);
    MM_A(6, 0);
    TS(44);
    cnt_wait(L_CNT + 4);
    f32x4 k0 = zero4, k1 = zero4;
#pragma unroll
    for (int d = 0; d < 8; ++d) {
      if (d == w) { k0 = acc[d >> 1][2 * (d & 1)]; k1 = acc[d >> 1][2 * (d & 1) + 1]; }
      else {
        const int i = (d - w - 1) & 7;
        LAS char* dst = lds + (i < 4 ? (unsigned)(w * 8192 + i * 2048) : i < 6 ? slot + (unsigned)((i - 4) * 2048) : L_G32 + (unsigned)(w * 6144)) + l16;
        *(LAS f32x4*)dst = acc[d >> 1][2 * (d & 1)];
        *(LAS f32x4*)(dst + 1024) = acc[d >> 1][2 * (d & 1) + 1];
      }
    }
    bar();
    TS(13);
#pragma unroll
    for (int i = 0; i < 7; ++i) {
      const int e = (w - 1 - i) & 7;
      LAS char* src = lds + (i < 4 ? (unsigned)(e * 8192 + i * 2048) : i < 6 ? L_SP + (unsigned)(e * 6144 + (i - 4) * 2048) : L_G32 + (unsigned)(e * 6144)) + l16;
      k0 += *(LAS f32x4*)src;
      k1 += *(LAS f32x4*)(src + 1024);
    }
    const f32x4 A = (g & 1) ? k1 : k0, B = (g & 1) ? k0 : k1;
    u32x4 o;
    o.x = pk_bf16(lo_f(hb.x) + A[0] + lo_f(bb2.x), hi_f(hb.x) + A[1] + hi_f(bb2.x));
    o.y = pk_bf16(lo_f(hb.y) + A[2] + lo_f(bb2.y), hi_f(hb.y) + A[3] + hi_f(bb2.y));
    o.z = pk_bf16(lo_f(hb.z) + B[0] + lo_f(bb2.z), hi_f(hb.z) + B[1] + hi_f(bb2.z));
    o.w = pk_bf16(lo_f(hb.w) + B[2] + lo_f(bb2.w), hi_f(hb.w) + B[3] + hi_f(bb2.w));
    const unsigned xo_off = (unsigned)(((tilef * 16 + r) * 512 + ct * 64 + 16 * g + 8 * khf) * 2);
    *(u32x4*)((char*)out + xo_off) = o;
  }
  TS(14);
}

extern "C" void kernel_launch(void* const* d_in, const int* in_sizes, int n_in, void* d_out, int out_size, void* d_ws, size_t ws_size, hipStream_t stream) {
  const bf16_t* x = (const bf16_t*)d_in[0]; const bf16_t* wqkv = (const bf16_t*)d_in[1]; const bf16_t* wo = (const bf16_t*)d_in[2];
  const bf16_t* w1 = (const bf16_t*)d_in[4]; const bf16_t* b1 = (const bf16_t*)d_in[5]; const bf16_t* w2 = (const bf16_t*)d_in[6]; const bf16_t* b2 = (const bf16_t*)d_in[7];
  (void)hipFuncSetAttribute((const void*)fused_block, hipFuncAttributeMaxDynamicSharedMemorySize, 163840);
  fused_block<<<256, 512, 163840, stream>>>(x, wqkv, wo, w1, b1, w2, b2, (char*)d_ws, (bf16_t*)d_out);
}
```

```cpp
#include <hip/hip_runtime.h>
#include <stdint.h>

typedef unsigned short bf16_t;
typedef unsigned long long u64;
typedef __attribute__((ext_vector_type(8))) short s16x8;
typedef __attribute__((ext_vector_type(4))) short s16x4;
typedef __attribute__((ext_vector_type(4))) float f32x4;
typedef __attribute__((ext_vector_type(2))) float f32x2;
typedef __attribute__((ext_vector_type(4))) unsigned u32x4;
typedef __attribute__((ext_vector_type(2))) unsigned u32x2;
#define LAS __attribute__((address_space(3)))
#define DI __device__ __forceinline__

constexpr unsigned WS_E    = 0;
constexpr unsigned WS_F1   = 65536;
constexpr unsigned WS_F2   = 262144;
constexpr unsigned WS_F3   = 524288;
constexpr unsigned WS_F4A  = 786432;
constexpr unsigned WS_F4B  = 1048576;
constexpr unsigned WS_KVP  = 1310720;
constexpr unsigned WS_O    = 2u << 20;
constexpr unsigned WS_H    = 4u << 20;
constexpr unsigned WS_HH   = 6u << 20;
constexpr unsigned WS_HS   = 14u << 20;
constexpr int KVP_STRIDE = 1152;

typedef __attribute__((ext_vector_type(2))) __bf16 bf16x2_t;
DI unsigned pk_bf16(float lo, float hi) { return __builtin_bit_cast(unsigned, __builtin_convertvector((f32x2){lo, hi}, bf16x2_t)); }
DI float lo_f(unsigned u) { return __uint_as_float(u << 16); }
DI float hi_f(unsigned u) { return __uint_as_float(u & 0xffff0000u); }

DI void glds16(const void* sbase, unsigned voff, unsigned lds_dst) {
  unsigned keep;
  asm volatile("s_mov_b32 %0, m0\n\ts_mov_b32 m0, %3\n\ts_nop 0\n\tglobal_load_lds_dwordx4 %1, %2\n\ts_mov_b32 m0, %0"
               : "=&s"(keep) : "v"(voff), "s"(sbase), "s"(lds_dst) : "memory");
}
#define GLD4(d, voff, sb, imm)  asm volatile("global_load_dwordx4 %0, %1, %2 offset:%3" : "=v"(d) : "v"(voff), "s"(sb), "i"(imm) : "memory")
#define GLD2(d, voff, sb, imm)  asm volatile("global_load_dwordx2 %0, %1, %2 offset:%3" : "=v"(d) : "v"(voff), "s"(sb), "i"(imm) : "memory")
#define GLD1(d, voff, sb, imm)  asm volatile("global_load_dword %0, %1, %2 offset:%3" : "=v"(d) : "v"(voff), "s"(sb), "i"(imm) : "memory")
#define GLD4S(d, voff, sb, imm) asm volatile("global_load_dwordx4 %0, %1, %2 offset:%3 sc1" : "=v"(d) : "v"(voff), "s"(sb), "i"(imm) : "memory")
#define GLD2S(d, voff, sb, imm) asm volatile("global_load_dwordx2 %0, %1, %2 offset:%3 sc1" : "=v"(d) : "v"(voff), "s"(sb), "i"(imm) : "memory")
#define GLD1S(d, voff, sb, imm) asm volatile("global_load_dword %0, %1, %2 offset:%3 sc1" : "=v"(d) : "v"(voff), "s"(sb), "i"(imm) : "memory")
#define WAIT_VM(n) asm volatile("s_waitcnt vmcnt(%0)" :: "i"(n) : "memory")
#define WAIT_VM1(n, a) asm volatile("s_waitcnt vmcnt(%1)" : "+v"(a) : "i"(n) : "memory")
#define WAIT_VM2(n, a, b) asm volatile("s_waitcnt vmcnt(%2)" : "+v"(a), "+v"(b) : "i"(n) : "memory")
#define WAIT_VM4(n, a, b, c, d) asm volatile("s_waitcnt vmcnt(%4)" : "+v"(a), "+v"(b), "+v"(c), "+v"(d) : "i"(n) : "memory")
#define WAIT_LGKM() asm volatile("s_waitcnt lgkmcnt(0)" ::: "memory")
#define MEMFENCE() asm volatile("" ::: "memory")
DI void bar() { asm volatile("s_waitcnt lgkmcnt(0)" ::: "memory"); __builtin_amdgcn_s_barrier(); asm volatile("" ::: "memory"); }
#define SCHED_FENCE() __builtin_amdgcn_sched_barrier(0)
#define RAW_BAR() do { asm volatile("" ::: "memory"); __builtin_amdgcn_s_barrier(); asm volatile("" ::: "memory"); } while (0)
#define ILVX(n, d, v) do { _Pragma("unroll") for (int z_ = 0; z_ < (n); ++z_) { __builtin_amdgcn_sched_group_barrier(0x008, 1, 0); if ((d) > 0) __builtin_amdgcn_sched_group_barrier(0x100, (d), 0); if ((v) > 0) __builtin_amdgcn_sched_group_barrier(0x002, (v), 0); } } while (0)

DI s16x8 tr_frag(LAS char* lds, unsigned a0, unsigned a1) {
  s16x4 a = __builtin_amdgcn_ds_read_tr16_b64_v4i16((LAS s16x4*)(lds + a0));
  s16x4 b = __builtin_amdgcn_ds_read_tr16_b64_v4i16((LAS s16x4*)(lds + a1));
  return __builtin_shufflevector(a, b, 0, 1, 2, 3, 4, 5, 6, 7);
}
DI f32x4 mfma32(s16x8 a, s16x8 b, f32x4 c) { return __builtin_amdgcn_mfma_f32_16x16x32_bf16(a, b, c, 0, 0, 0); }
DI f32x4 mfma16(s16x4 a, s16x4 b, f32x4 c) { return __builtin_amdgcn_mfma_f32_16x16x16bf16_1k(a, b, c, 0, 0, 0); }
DI f32x4 mfma4(float a, float b, f32x4 c) { return __builtin_amdgcn_mfma_f32_16x16x4f32(a, b, c, 0, 0, 0); }
DI float phi(float t) { return fmaxf(t, 0.f) + __builtin_amdgcn_fmed3f(__builtin_amdgcn_exp2f(t * 1.44269504089f), 0.f, 1.f) + 1e-6f; }
DI s16x8 as_frag(u32x4 v) { return __builtin_bit_cast(s16x8, v); }
DI u32x4 as_u4(s16x8 v) { return __builtin_bit_cast(u32x4, v); }
DI f32x4 as_f4(u32x4 v) { return __builtin_bit_cast(f32x4, v); }
DI float diag4(f32x4 d, int r) { const float t = (r & 1) ? d[1] : d[0], u = (r & 1) ? d[3] : d[2]; return (r & 2) ? u : t; }

typedef __amdgpu_buffer_rsrc_t rsrc_t;
DI void st16_sc1(rsrc_t rs, unsigned off, u32x4 v) { __builtin_amdgcn_raw_buffer_store_b128(v, rs, off, 0, 16); }
DI void st8_sc1(rsrc_t rs, unsigned off, u32x2 v) { __builtin_amdgcn_raw_buffer_store_b64(v, rs, off, 0, 16); }

DI void wait_flags(const char* ws, unsigned off, unsigned tlo, unsigned thi) {
#pragma nounroll
  for (int it = 0; it < (1 << 18); ++it) {
    u32x2 v;
    asm volatile("global_load_dwordx2 %0, %1, %2 sc1\n\ts_waitcnt vmcnt(0)" : "=v"(v) : "v"(off), "s"(ws) : "memory");
    const bool ok = (v.x == tlo) & (v.y == thi);
    if (__builtin_amdgcn_ballot_w64(ok) == ~0ull) break;
    __builtin_amdgcn_s_sleep(1);
  }
  asm volatile("" ::: "memory");
}

#define TS(i)

DI unsigned g64_rd(int g, int q, int pp, int u, int b) {
  int rho = 4 * g + q;
  return (unsigned)(128 * rho + 16 * ((2 * pp + u) ^ ((rho >> 1) & 3)) + 8 * (b ^ (pp & 1)));
}
DI f32x2 gelu2(f32x2 v) {
  const f32x2 t = v * v;
  const f32x2 a = v * (t * (f32x2){-0.1029432397f, -0.1029432397f} + (f32x2){-2.3022082f, -2.3022082f});
  const f32x2 d = (f32x2){__builtin_amdgcn_exp2f(a.x), __builtin_amdgcn_exp2f(a.y)} + (f32x2){1.f, 1.f};
  return v * (f32x2){__builtin_amdgcn_rcpf(d.x), __builtin_amdgcn_rcpf(d.y)};
}
DI int hcol(int cp, int j) {
  if (j < 64) return 64 * cp + j;
  const int sw = (cp & 3) == 1;
  return 1024 + 48 * cp + (j < 96 ? (j - 64) + (sw ? 16 : 0) : (j - 96) + (sw ? 0 : 32));
}
DI int w2_row(int e, int n, int rho) {
  const int gg = (rho >> 2) & 3, j = 4 * (rho >> 4) + (rho & 3);
  if (n < 6) { const int cp = 2 * e + (n & 1), u = n >> 1, jj = j ^ (4 * (gg & 1)); return u < 2 ? hcol(cp, 16 * gg + 8 * u + jj) : hcol(cp, 64) + 8 * gg + jj; }
  return hcol(2 * e + (j >> 2), 96) + 4 * gg + (j & 3);
}
#define CODE_LINES 0
#define QG_ISSUE 0
#define QG_FLAG 6
#define P2ILV()
#define USE_T1 1
#define TI 2
#define NT 2
#define T4A 1
#define T4B 1
#define QG_G64 8
__global__ __launch_bounds__(512, 2) void fused_block(const bf16_t* __restrict__ x, const bf16_t* __restrict__ wqkv, const bf16_t* __restrict__ wo,
                                                      const bf16_t* __restrict__ w1, const bf16_t* __restrict__ b1, const bf16_t* __restrict__ w2,
                                                      const bf16_t* __restrict__ b2, char* ws, bf16_t* __restrict__ out) {
  extern __shared__ __attribute__((aligned(16))) char smem_[];
  LAS char* lds = (LAS char*)smem_;
  asm volatile("" :: "s"(x), "s"(wqkv), "s"(wo), "s"(w1), "s"(b1), "s"(w2), "s"(b2), "s"(ws), "s"(out));
  const int tid = threadIdx.x, lane = tid & 63;
  const int w = __builtin_amdgcn_readfirstlane(tid >> 6);
  const int bid = blockIdx.x;
  const int P = (bid & 7) * 2 + (bid >> 7), C = (bid >> 3) & 15, J = bid >> 3;
  const int g = lane >> 4, r = lane & 15, q = (lane >> 2) & 3, pp = lane & 3;
  const unsigned ldsb = (unsigned)(size_t)smem_;
  const rsrc_t rs = __builtin_amdgcn_make_buffer_rsrc(ws, 0, 0x7fffffff, 0x00020000);
  const unsigned l16 = (unsigned)lane * 16u;
  TS(0);
  const f32x4 zero4 = (f32x4){0, 0, 0, 0};
  const int ct = C & 7, th = C >> 3;
  constexpr unsigned L_WKV = 0, L_X = 65536, L_CS = 16384, L_T = 53248, L_G32 = 65536, L_SP = 114688;
  s16x8 ones; for (int j = 0; j < 8; ++j) ones[j] = (short)0x3f80;

  auto issue_w1p = [&](int kb, int pc) {
    auto srow = [&](int rho) { return 64 * (kb >> 1) + 16 * ((rho >> 2) & 3) + 8 * (kb & 1) + 4 * (rho >> 4) + (rho & 3); };
    if (pc < 4) { int rho = pc * 8 + (lane >> 3); glds16(w1, (unsigned)(srow(rho) * 3584 + C * 128 + (((lane & 7) ^ ((rho >> 1) & 3)) * 16)), ldsb + kb * 4096 + pc * 1024); }
    else if (pc < 6) { int rho = (pc - 4) * 16 + (lane >> 2); glds16(w1, (unsigned)(srow(rho) * 3584 + hcol(C, 64) * 2 + (((lane & 3) ^ ((rho >> 2) & 1)) * 16)), ldsb + L_G32 + kb * 3072 + (pc - 4) * 1024); }
    else { int rho = lane >> 1; glds16(w1, (unsigned)(srow(rho) * 3584 + hcol(C, 96) * 2 + (lane & 1) * 16), ldsb + L_G32 + kb * 3072 + 2048); }
  };
  unsigned rd4[4];
#pragma unroll
  for (int t = 0; t < 4; ++t) rd4[t] = g64_rd(g, q, pp, t >> 1, t & 1);
  auto lin64 = [&](int kb) {
    s16x8 f[4];
#pragma unroll
    for (int i = 0; i < 4; ++i) { const unsigned a = (unsigned)(kb * 4096) + rd4[i]; f[i] = tr_frag(lds, a, a + 2048); }
    WAIT_LGKM();
#pragma unroll
    for (int i = 0; i < 4; ++i) *(LAS s16x8*)(lds + kb * 4096 + i * 1024 + l16) = f[i];
  };
  const int m2 = w >> 2, kq = w & 3;

  u32x4 pq;
  unsigned tlo, thi;
  u32x4 xr = (u32x4){0, 0, 0, 0};
  const unsigned xs = L_X + (unsigned)(w * 12288);
  unsigned tw0 = 0, tw1 = 0;
  u32x4 ga[16], gz;
  const char* kb0 = ws + WS_KVP + (unsigned)((C * 2 + (w & 1)) * 16 * KVP_STRIDE);
  auto sweep_issue = [&]() {
    const unsigned zoff = (unsigned)(r * KVP_STRIDE + 1024 + g * 16);
#pragma unroll
    for (int i = 0; i < 16; ++i) GLD4S(ga[i], l16, kb0 + (i >> 1) * 2 * KVP_STRIDE, (i & 1) * KVP_STRIDE);
    GLD4S(gz, zoff, kb0, 0);
  };
  {
    u32x2 Ev = (u32x2){0u, 0u};
    u32x4 xf[16];
    const int xrow = lane >> 3;
    const unsigned xoff = (unsigned)(((P * 128 + w * 16 + xrow) * 512) * 2 + (((lane & 7) ^ (xrow & 7)) * 16));
    const int wk = lane >> 2;
    const unsigned wcol = (unsigned)((((((lane >> 1) & 1) ^ ((wk >> 3) & 1)) * 16) + (lane & 1) * 8) * 2);
    const unsigned woff = (unsigned)(wk * 1536 * 2) + wcol + (unsigned)(C * 64);
    auto issue_x = [&](int ch) {
      const unsigned dst = ldsb + xs + (ch % 6) * 2048;
      glds16(x, xoff + ch * 128, dst);
      glds16(x, xoff + ch * 128 + 8 * 1024, dst + 1024);
    };
    auto issue_wkv = [&](int ch) {
      int sub = w >> 1, seg = w & 1;
      glds16(wqkv, woff + (unsigned)((ch * 64 + sub * 16) * 3072 + (1 + seg) * 1024), ldsb + L_WKV + ch * 8192 + w * 1024);
    };
    auto issue_wq = [&](int i) {
      int blk = w * 4 + i;
      glds16(wqkv, woff + (unsigned)(blk * 16 * 3072), ldsb + xs + 4096 + i * 1024);
    };
    const int worow = lane >> 3;
    const unsigned wooff = (unsigned)(worow * 1024 + ct * 128 + (((lane & 7) ^ ((worow >> 1) & 3)) * 16));
    auto issue_wo = [&](int i) { int blk = w * 8 + i; glds16(wo, wooff + blk * 8192, ldsb + xs + (i < 4 ? 8192 + i * 1024 : (i - 4) * 1024)); };
    unsigned tc0 = 0, tc1 = 0;
    if (w == 7) asm volatile("global_load_dwordx2 %0, %1, %2 sc1" : "=v"(Ev) : "v"(WS_E), "s"(ws) : "memory");
#pragma unroll
    for (int ch = 0; ch < 8; ++ch) {
      issue_wkv(ch);
      if (ch < 6) issue_x(ch);
      if (ch == 1) {
        RAW_BAR();
        if (w < 6 && USE_T1) {
          if (lane < 32) GLD1(tc0, (unsigned)((w + 2) * 64 + 2 * J + g) * 3072u + 1024u + (unsigned)r * 128u, wqkv, 0);
          else if (lane < 40) GLD1(tc1, (unsigned)((bid & 7) * 256 + J * 8 + (lane - 32)) * 1024u + (unsigned)(w + 2) * 128u, x, 0);
        } else if (lane < 32) {
          const unsigned row = (unsigned)(J * 16 + (w & 1) * 8 + (lane >> 3));
          GLD1(tc0, row * 3072u + (unsigned)(lane & 7) * 128u, wqkv, 0);
          GLD1(tc1, (row + 4u) * 3072u + (unsigned)(lane & 7) * 128u, wqkv, 0);
        }
      }
      if (ch == 3) RAW_BAR();
    }
    TS(53);
    const unsigned wrd = (unsigned)((g >> 1) * 1024 + (8 * (g & 1) + q) * 64 + 8 * pp);
    const unsigned wrd0 = L_WKV + (g >> 1) * 1024 + wrd + 32u * (0 ^ (g & 1)), wrd1 = L_WKV + (g >> 1) * 1024 + wrd + 32u * (1 ^ (g & 1));
    const unsigned wrdc = ((w & 2) ? wrd1 : wrd0) + (unsigned)((w >> 2) * 4096 + (w & 1) * 1024);
    const unsigned xrd0 = xs + r * 128 + (unsigned)(((0 + g) ^ (r & 7)) * 16), xrd1 = xs + r * 128 + (unsigned)(((4 + g) ^ (r & 7)) * 16);
    const unsigned xrr = L_X + (unsigned)((4 * th + (w >> 1)) * 12288 + (ct % 6) * 2048 + r * 128 + (((2 * g + (w & 1)) ^ (r & 7)) * 16));

    s16x8 wf[2][2][4];
    s16x8 cf, cf7;
    f32x4 acc[4], cl = zero4, sa = zero4, qa = zero4;
    acc[0] = acc[1] = acc[2] = acc[3] = zero4;
#pragma unroll
    for (int ch = 0; ch <= 8; ++ch) {
      if (ch < 8) {
        if (ch == 2) WAIT_VM2(13, tc0, tc1);
        else WAIT_VM(ch == 0 ? 19 : ch == 1 ? 16 : ch == 3 ? 12 : ch == 4 ? 11 : ch == 5 ? 10 + NT : ch == 6 ? 6 + NT : 4 + NT);
        xf[2 * ch] = *(LAS u32x4*)(lds + xrd0 + (ch % 6) * 2048);
        xf[2 * ch + 1] = *(LAS u32x4*)(lds + xrd1 + (ch % 6) * 2048);
        bar();
        if (ch == 0) TS(45); if (ch == 2) TS(47); if (ch == 4) TS(49); if (ch == 6) TS(51); if (ch == 7) TS(52);
        if (ch == 1 || ch == 2) issue_x(5 + ch);
        else if (ch == 3 || ch == 4) { issue_wq(2 * (ch - 3)); issue_wq(2 * (ch - 3) + 1); }
        if (ch == 4 && NT > 0) {
          const unsigned idx = (unsigned)((J * 8 + w) * 16 + r), i8 = idx >> 3, lo = (idx & 7u) * 128u;
          const char* ta = g == 0 ? (const char*)wo + idx * 128u
                                  : (const char*)w2 + ((16u * (i8 >> 3) + (i8 & 7u)) * 1024u + lo);
          if (lane < (T4A ? 32 : 16)) asm volatile("global_load_dword %0, %1, off" : "=v"(tw0) : "v"(ta) : "memory");
          GLD1(tw1, (unsigned)((J * 8 + w) * 56 + (lane < 56 ? lane : 55)) * 128u, w1, 0);
        }
        if (ch == ct) xr = *(LAS u32x4*)(lds + xrr);
        SCHED_FENCE();
#pragma unroll
        for (int s = 0; s < 2; ++s)
#pragma unroll
          for (int t = 0; t < 4; ++t) {
            const unsigned a = ((t >> 1) ? wrd1 : wrd0) + ch * 8192 + s * 4096 + (t & 1) * 1024;
            wf[ch & 1][s][t] = tr_frag(lds, a, a + 256);
          }
      }
      if (ch > 0) {
        const int c0 = ch - 1;
        if (c0 < 6) cl = mfma32(ones, cf, cl);
#pragma unroll
        for (int s = 0; s < 2; ++s) {
          const s16x8 xa = as_frag(xf[c0 * 2 + s]);
#pragma unroll
          for (int t = 0; t < 4; ++t) acc[t] = mfma32(xa, wf[c0 & 1][s][t], acc[t]);
          sa = mfma32(ones, xa, sa);
          qa = mfma32(xa, xa, qa);
        }
        if (ch < 8) ILVX(8, 2, 0);
      }
      if (ch < 7) cf = tr_frag(lds, wrdc + ch * 8192, wrdc + ch * 8192 + 256);
      if (ch == 6) {
        cf7 = tr_frag(lds, wrdc + 7 * 8192, wrdc + 7 * 8192 + 256);
        cl = mfma32(ones, cf, cl);
        cl = mfma32(ones, cf7, cl);
        if (g == 0) *(LAS float*)(lds + L_CS + r * 32 + w * 4) = cl[0];
      }
      SCHED_FENCE();
    }
    TS(1);
    asm volatile("" : "+v"(Ev));
    if (w == 7 && lane == 0) *(LAS u32x2*)(lds + L_T + 3584) = Ev;
    const unsigned a0 = L_X + 4096 + wrd + 32u * (0 ^ (g & 1)), a1 = L_X + 4096 + wrd + 32u * (1 ^ (g & 1));
    float mean1, rstd1;
    {
      const float qd = (g == (r >> 2)) ? diag4(qa, r) : 0.f;
      const f32x4 Sr = mfma4(sa[0], 0.25f, zero4), Qr = mfma4(qd, 1.f, zero4);
      const f32x4 cs = *(LAS f32x4*)(lds + L_CS + r * 32) + *(LAS f32x4*)(lds + L_CS + r * 32 + 16);
      const float Qc = mfma4(1.f, qd, zero4)[0];
      float val[4][4];
#pragma unroll
      for (int j = 0; j < 4; ++j) {
        const float mu = Sr[j] * (1.f / 512.f); const float var = fmaxf(Qr[j] * (1.f / 512.f) - mu * mu, 0.f); const float rsd = __builtin_amdgcn_rsqf(var + 1e-5f);
#pragma unroll
        for (int t = 0; t < 4; ++t) { const float v = rsd * (acc[t][j] - mu * cs[t]); val[t][j] = (t & 1) ? v : phi(v); }
      }
      s16x4 one4; for (int j = 0; j < 4; ++j) one4[j] = (short)0x3f80;
#pragma unroll
      for (int hd = 0; hd < 2; ++hd) {
        const s16x4 pk4 = __builtin_bit_cast(s16x4, (u32x2){pk_bf16(val[2 * hd][0], val[2 * hd][1]), pk_bf16(val[2 * hd][2], val[2 * hd][3])});
        const s16x4 v4 = __builtin_bit_cast(s16x4, (u32x2){pk_bf16(val[2 * hd + 1][0], val[2 * hd + 1][1]), pk_bf16(val[2 * hd + 1][2], val[2 * hd + 1][3])});
        const f32x4 kvp = mfma16(pk4, v4, zero4), zp = mfma16(pk4, one4, zero4);
        *(LAS f32x4*)(lds + hd * 8192 + w * 1024 + l16) = kvp;
        if (r == 0) *(LAS f32x4*)(lds + L_T + 1024 + (hd * 8 + w) * 64 + g * 16) = zp;
      }
      mean1 = sa[0] * (1.f / 512.f); rstd1 = __builtin_amdgcn_rsqf(fmaxf(Qc * (1.f / 512.f) - mean1 * mean1, 0.f) + 1e-5f);
    }
    SCHED_FENCE();
    TS(22);
    bar();
    TS(24);
    {
      Ev = *(LAS u32x2*)(lds + L_T + 3584);
      const u64 E = ((u64)(unsigned)__builtin_amdgcn_readfirstlane(Ev.y) << 32) | (unsigned)__builtin_amdgcn_readfirstlane(Ev.x);
      const u64 T = E * 0x9E3779B97F4A7C15ull + 0xD1B54A32D192ED03ull;
      tlo = (unsigned)T; thi = (unsigned)(T >> 32);
    }
    if (w < 2) {
      f32x4 s = zero4, sz = zero4;
#pragma unroll
      for (int mm = 0; mm < 8; ++mm) {
        s += *(LAS f32x4*)(lds + w * 8192 + mm * 1024 + l16);
        sz += *(LAS f32x4*)(lds + L_T + 1024 + (w * 8 + mm) * 64 + (lane & 3) * 16);
      }
      const unsigned dst = WS_KVP + (unsigned)(((C * 2 + w) * 16 + P) * KVP_STRIDE) + l16;
      st16_sc1(rs, dst, (u32x4){pk_bf16(s[0], s[1]), tlo, pk_bf16(s[2], s[3]), tlo});
      if (lane < 4) st16_sc1(rs, dst + 1024, (u32x4){pk_bf16(sz[0], sz[1]), tlo, pk_bf16(sz[2], sz[3]), tlo});
      WAIT_VM(2 + NT);
    } else WAIT_VM(NT);
    {
      const unsigned o = (unsigned)(w * 12288);
      f32x4 c0 = mfma32(tr_frag(lds, a0 + o, a0 + o + 256), ones, zero4), c1 = mfma32(tr_frag(lds, a1 + o, a1 + o + 256), ones, zero4);
      c0 = mfma32(tr_frag(lds, a0 + o + 2048, a0 + o + 2304), ones, c0); c1 = mfma32(tr_frag(lds, a1 + o + 2048, a1 + o + 2304), ones, c1);
      if (r == 0) { *(LAS f32x4*)(lds + L_T + w * 128 + g * 32) = c0; *(LAS f32x4*)(lds + L_T + w * 128 + g * 32 + 16) = c1; }
    }
    bar();
    TS(23);
    f32x4 aq[2];
    aq[0] = aq[1] = zero4;
    {
      s16x8 f[2][4];
#pragma unroll
      for (int kk = 0; kk <= 8; ++kk) {
        if (kk < 8) {
#pragma unroll
          for (int s = 0; s < 2; ++s) {
            f[kk & 1][s * 2 + 0] = tr_frag(lds, a0 + kk * 12288 + s * 2048, a0 + kk * 12288 + s * 2048 + 256);
            f[kk & 1][s * 2 + 1] = tr_frag(lds, a1 + kk * 12288 + s * 2048, a1 + kk * 12288 + s * 2048 + 256);
          }
        }
        if (kk > 0) {
          const int k0 = kk - 1;
#pragma unroll
          for (int s = 0; s < 2; ++s) {
            aq[0] = mfma32(f[k0 & 1][s * 2 + 0], as_frag(xf[k0 * 2 + s]), aq[0]);
            aq[1] = mfma32(f[k0 & 1][s * 2 + 1], as_frag(xf[k0 * 2 + s]), aq[1]);
          }
          if (kk < 8) ILVX(4, 2, 0);
        }
        SCHED_FENCE();
        if (kk == QG_ISSUE) {
#pragma unroll
          for (int i = 0; i < 8; ++i) issue_wo(i);
          SCHED_FENCE();
        }
        if (kk == QG_G64) {
          WAIT_VM2(8, tw0, tw1);
          if ((w >> 1) == 1) sweep_issue();
          else {
#pragma unroll
            for (int i = 0; i < 4; ++i) issue_w1p(2 * w, i);
            if (w != 6) {
#pragma unroll
              for (int i = 0; i < 4; ++i) issue_w1p(2 * w + 1, i);
            }
          }
          SCHED_FENCE();
        }
      }
    }
    TS(25);
    {
      f32x4 c0 = zero4, c1 = zero4;
#pragma unroll
      for (int ww = 0; ww < 8; ++ww) { c0 += *(LAS f32x4*)(lds + L_T + ww * 128 + g * 32); c1 += *(LAS f32x4*)(lds + L_T + ww * 128 + g * 32 + 16); }
      float a[4], b[4];
#pragma unroll
      for (int j = 0; j < 4; ++j) { a[j] = phi(rstd1 * (aq[0][j] - mean1 * c0[j])); b[j] = phi(rstd1 * (aq[1][j] - mean1 * c1[j])); }
      pq.x = pk_bf16(a[0], a[1]); pq.y = pk_bf16(a[2], a[3]); pq.z = pk_bf16(b[0], b[1]); pq.w = pk_bf16(b[2], b[3]);
    }
    asm volatile("" : "+v"(pq), "+v"(xr));
  }
  TS(4);
  if ((w >> 1) == 1) {
    const int hd = w & 1;
    f32x4 s, z;
    auto sweep_check = [&]() -> bool {
      unsigned bad = 0;
      s = zero4;
#pragma unroll
      for (int i = 0; i < 16; i += 4) {
        WAIT_VM4(13 - i, ga[i], ga[i + 1], ga[i + 2], ga[i + 3]);
#pragma unroll
        for (int k = i; k < i + 4; ++k) {
          bad |= (ga[k].y ^ tlo) | (ga[k].w ^ tlo);
          s += (f32x4){lo_f(ga[k].x), hi_f(ga[k].x), lo_f(ga[k].z), hi_f(ga[k].z)};
        }
      }
      WAIT_VM1(0, gz);
      bad |= (gz.y ^ tlo) | (gz.w ^ tlo);
      z = (f32x4){lo_f(gz.x), hi_f(gz.x), lo_f(gz.z), hi_f(gz.z)};
      return __builtin_amdgcn_ballot_w64(bad != 0u) == 0ull;
    };
    if (!sweep_check()) {
#pragma nounroll
      for (int it = 0; it < (1 << 16); ++it) {
        TS(3);
        sweep_issue();
        if (sweep_check()) break;
      }
    }
#pragma unroll
    for (int i = 0; i < 8; ++i) issue_w1p(2 * w + (i >> 2), i & 3);
    TS(5);
    *(LAS u32x2*)(lds + L_T + 2048 + hd * 1024 + lane * 8) = (u32x2){pk_bf16(s[0], s[1]), pk_bf16(s[2], s[3])};
#pragma unroll
    for (int j = 0; j < 4; ++j) {
      float t = z[j];
      t += __builtin_bit_cast(float, __builtin_amdgcn_update_dpp(0, __builtin_bit_cast(int, t), 0x128, 0xf, 0xf, false));
      t += __builtin_bit_cast(float, __builtin_amdgcn_update_dpp(0, __builtin_bit_cast(int, t), 0x124, 0xf, 0xf, false));
      t += __builtin_bit_cast(float, __builtin_amdgcn_update_dpp(0, __builtin_bit_cast(int, t), 0x122, 0xf, 0xf, false));
      t += __builtin_bit_cast(float, __builtin_amdgcn_update_dpp(0, __builtin_bit_cast(int, t), 0x121, 0xf, 0xf, false));
      z[j] = t;
    }
    *(LAS u32x2*)(lds + L_T + 2048 + hd * 1024 + 512 + lane * 8) = (u32x2){pk_bf16(z[0], z[1]), pk_bf16(z[2], z[3])};
  } else {
    WAIT_VM(0);
    lin64(2 * w);
    if (w != 6) lin64(2 * w + 1);
  }
  TS(29);
  bar();
  {
    u32x4 o;
#pragma unroll
    for (int hd = 0; hd < 2; ++hd) {
      const s16x4 kvA = *(LAS s16x4*)(lds + L_T + 2048 + hd * 1024 + lane * 8);
      const s16x4 zA = *(LAS s16x4*)(lds + L_T + 2048 + hd * 1024 + 512 + lane * 8);
      u32x2 pu; pu.x = hd ? pq.z : pq.x; pu.y = hd ? pq.w : pq.y;
      const s16x4 p4 = __builtin_bit_cast(s16x4, pu);
      const f32x4 num = mfma16(kvA, p4, zero4);
      const f32x4 den = mfma16(zA, p4, zero4);
      const float inv = __builtin_amdgcn_rcpf(den[0] + 1e-6f);
      if (hd == 0) { o.x = pk_bf16(num[0] * inv, num[1] * inv); o.y = pk_bf16(num[2] * inv, num[3] * inv); }
      else { o.z = pk_bf16(num[0] * inv, num[1] * inv); o.w = pk_bf16(num[2] * inv, num[3] * inv); }
    }
    st16_sc1(rs, WS_O + (unsigned)(((P * 8 + w) * 16 + C) * 1024) + l16, o);
  }
  SCHED_FENCE();
  TS(30);
  s16x8 wof[4][4];
#pragma unroll
  for (int i = 0; i < 4; ++i)
#pragma unroll
    for (int t = 0; t < 4; ++t) { const unsigned a = L_X + (unsigned)((kq * 2 + (i >> 1)) * 12288 + ((i & 1) ? 0 : 8192)) + rd4[t]; wof[i][t] = tr_frag(lds, a, a + 2048); }
  SCHED_FENCE();
  WAIT_VM(0);
  if (lane == 0) st8_sc1(rs, WS_F2 + (unsigned)(((P * 8 + w) * 16 + C) * 128), (u32x2){tlo, thi});
  TS(6);
  bar();
  const int tile0 = P * 8 + th * 4 + 2 * m2;
  const int tilef = tile0 + (kq >> 1), khf = kq & 1;
  u32x4 bb2;
  u32x2 bb1;
  if ((w >> 1) == 1) { lin64(2 * w); lin64(2 * w + 1); }
  if (w == 6) {
#pragma unroll
    for (int i = 0; i < 4; ++i) issue_w1p(13, i);
  }
#pragma unroll
  for (int i = 0; i < 6; ++i) issue_w1p(2 * w + (i / 3), 4 + i % 3);
  {
    const unsigned b2o = (unsigned)((ct * 64 + 16 * g + 8 * khf) * 2);
    GLD4(bb2, b2o, b2, 0);
    const int wt = w < 7 ? w : 6;
    const int col = wt < 4 ? 16 * g + 8 * (wt >> 1) + 4 * ((wt & 1) ^ (g & 1)) : wt < 6 ? 64 + 8 * g + 4 * ((wt & 1) ^ (g & 1)) : 96 + 4 * g;
    const unsigned b1o = (unsigned)(hcol(C, col) * 2);
    GLD2(bb1, b1o, b1, 0);
  }
  TS(28);
  wait_flags(ws, WS_F2 + (unsigned)(((tile0 + ((lane >> 2) & 1)) * 16 + 4 * kq + (lane & 3)) * 128), tlo, thi);
  TS(7);
  asm volatile("" : "+v"(bb2), "+v"(bb1));
  u32x4 hb;
  {
    u32x4 of[2][4];
    const char* ob = ws + WS_O + (unsigned)((tile0 * 16 + kq * 4) * 1024);
#pragma unroll
    for (int i = 0; i < 4; ++i)
#pragma unroll
      for (int tt = 0; tt < 2; ++tt) GLD4S(of[tt][i], l16, ob + tt * 16384, i * 1024);
    f32x4 acc[2][4];
#pragma unroll
    for (int t = 0; t < 4; ++t) acc[0][t] = acc[1][t] = zero4;
#pragma unroll
    for (int i = 0; i < 4; ++i) {
      WAIT_VM2(6 - 2 * i, of[0][i], of[1][i]);
#pragma unroll
      for (int tt = 0; tt < 2; ++tt)
#pragma unroll
        for (int t = 0; t < 4; ++t) acc[tt][t] = mfma32(wof[i][t], as_frag(of[tt][i]), acc[tt][t]);
      SCHED_FENCE();
    }
    f32x4 k0 = zero4, k1 = zero4;
#pragma unroll
    for (int d = 0; d < 4; ++d) {
      if (d == kq) { k0 = acc[d >> 1][2 * (d & 1)]; k1 = acc[d >> 1][2 * (d & 1) + 1]; }
      else {
        LAS char* dst = lds + L_SP + ((m2 * 4 + d) * 3 + ((kq - d - 1) & 3)) * 2048 + l16;
        *(LAS f32x4*)dst = acc[d >> 1][2 * (d & 1)];
        *(LAS f32x4*)(dst + 1024) = acc[d >> 1][2 * (d & 1) + 1];
      }
    }
    TS(32);
    bar();
    TS(33);
#pragma unroll
    for (int s = 0; s < 3; ++s) {
      k0 += *(LAS f32x4*)(lds + L_SP + (w * 3 + s) * 2048 + l16);
      k1 += *(LAS f32x4*)(lds + L_SP + (w * 3 + s) * 2048 + 1024 + l16);
    }
    const f32x4 A = (g & 1) ? k1 : k0, B = (g & 1) ? k0 : k1;
    hb.x = pk_bf16(lo_f(xr.x) + A[0], hi_f(xr.x) + A[1]); hb.y = pk_bf16(lo_f(xr.y) + A[2], hi_f(xr.y) + A[3]);
    hb.z = pk_bf16(lo_f(xr.z) + B[0], hi_f(xr.z) + B[1]); hb.w = pk_bf16(lo_f(xr.w) + B[2], hi_f(xr.w) + B[3]);
    const unsigned blk = (unsigned)(tilef * 16 + 2 * ct + khf);
    st16_sc1(rs, WS_H + blk * 1024 + l16, hb);
    const f32x4 sS = mfma32(ones, as_frag(hb), zero4), sQ = mfma32(as_frag(hb), as_frag(hb), zero4);
    if (g == (r >> 2)) st8_sc1(rs, WS_HS + blk * 128 + (unsigned)r * 8u, (u32x2){__float_as_uint(sS[0]), __float_as_uint(diag4(sQ, r))});
  }
  TS(34);
  WAIT_VM(0);
  if (lane == 0) {
    st8_sc1(rs, WS_F3 + (unsigned)((tilef * 16 + 2 * ct + khf) * 128), (u32x2){tlo, thi});
    if (bid == 0 && w == 0) st8_sc1(rs, WS_E, (u32x2){tlo, thi});
  }
  TS(8);
  const unsigned slot = L_SP + (unsigned)(w * 6144);
  constexpr unsigned L_CNT = L_SP + 7 * 6144 + 4096;
  auto issue_w2 = [&](int n, unsigned dst) {
#pragma unroll
    for (int b = 0; b < 4; ++b) {
      const int rho = b * 8 + (lane >> 3);
      glds16(w2, (unsigned)(w2_row(w, n, rho) * 1024 + ct * 128 + (((lane & 7) ^ ((rho >> 1) & 3)) * 16)), ldsb + dst + b * 1024);
    }
  };
  s16x8 wr[4][4];
  auto read_w2 = [&](s16x8* f, unsigned src) {
#pragma unroll
    for (int t = 0; t < 4; ++t) f[t] = tr_frag(lds, src + rd4[t], src + rd4[t] + 2048);
  };
  auto cnt_add = [&](unsigned a) { if (lane == 0) __hip_atomic_fetch_add((LAS unsigned*)(lds + a), 1u, __ATOMIC_RELAXED, __HIP_MEMORY_SCOPE_WORKGROUP); MEMFENCE(); };
  auto cnt_wait = [&](unsigned a) {
#pragma nounroll
    for (int it = 0; it < (1 << 20); ++it) {
      MEMFENCE();
      const unsigned v = *(volatile LAS unsigned*)(lds + a);
      if (__builtin_amdgcn_readfirstlane(v) >= 8u) break;
      __builtin_amdgcn_s_sleep(1);
    }
    MEMFENCE();
  };
  issue_w2(0, slot);
  unsigned tw3 = 0;
  {
    const unsigned k = (unsigned)(40 * J + 5 * w) + (unsigned)((lane < 40 ? lane : 39) >> 3);
    GLD1(tw3, (k < 512u ? 16u * (k >> 3) + 8u + (k & 7u) : k + 512u) * 1024u + (unsigned)(lane & 7) * 128u, w2, 0);
  }
  {
    const int rho0 = 4 * g + q;
    s16x8 f[6];
#pragma unroll
    for (int i = 0; i < 2; ++i) {
      const unsigned base = L_G32 + (unsigned)((2 * w + i) * 3072);
#pragma unroll
      for (int b = 0; b < 2; ++b) { const unsigned a = base + (unsigned)(64 * rho0 + 16 * (pp ^ ((rho0 >> 2) & 1)) + 8 * (b ^ (pp & 1))); f[i * 3 + b] = tr_frag(lds, a, a + 1024); }
      const unsigned a = base + 2048u + (unsigned)(32 * rho0 + 8 * pp);
      f[i * 3 + 2] = tr_frag(lds, a, a + 512);
    }
    WAIT_LGKM();
#pragma unroll
    for (int i = 0; i < 6; ++i) *(LAS s16x8*)(lds + L_G32 + w * 6144 + i * 1024 + l16) = f[i];
  }
  if (w == 6) lin64(13);
  bar();
  TS(35);
  if (w < 7) {
    const unsigned a0 = (w < 4 ? (unsigned)(w * 1024) : L_G32 + (unsigned)((w - 4) * 1024)) + l16, st = w < 4 ? 4096u : 3072u;
    f32x4 c0 = zero4, c1 = zero4;
#pragma unroll
    for (int i = 0; i < 16; i += 2) {
      c0 = mfma32(*(LAS s16x8*)(lds + a0 + i * st), ones, c0);
      c1 = mfma32(*(LAS s16x8*)(lds + a0 + (i + 1) * st), ones, c1);
    }
    if (r == 0) {
      *(LAS f32x4*)(lds + slot + 4096 + g * 32) = c0 + c1;
      *(LAS f32x4*)(lds + slot + 4096 + g * 32 + 16) = (f32x4){lo_f(bb1.x), hi_f(bb1.x), lo_f(bb1.y), hi_f(bb1.y)};
    }
  } else if (lane == 0) *(LAS u32x2*)(lds + L_CNT) = (u32x2){0u, 0u};
  WAIT_VM(T4B);
  read_w2(wr[0], slot);
  bar();
  issue_w2(1, slot);
  TS(36);
  const int tile3 = P * 8 + w;
  wait_flags(ws, WS_F3 + (unsigned)((tile3 * 16 + r) * 128), tlo, thi);
  TS(9);
  asm volatile("" : "+v"(tw3));
  {
    u32x4 hf[16];
    u32x2 sv[4];
    const char* hbp = ws + WS_H + (unsigned)(tile3 * 16384);
    {
      const char* sp = ws + WS_HS + (unsigned)(tile3 * 2048);
      const unsigned l8 = (unsigned)lane * 8u;
#pragma unroll
      for (int i = 0; i < 4; ++i) GLD2S(sv[i], l8, sp, i * 512);
    }
#pragma unroll
    for (int i = 0; i < 16; ++i) GLD4S(hf[i], l16, hbp + (i >> 2) * 4096, (i & 3) * 1024);
    read_w2(wr[1], slot);
    if (w < 4) __builtin_amdgcn_s_setprio(3); else __builtin_amdgcn_s_setprio(2);
    f32x4 acc[7];
#pragma unroll
    for (int t = 0; t < 7; ++t) acc[t] = zero4;
    {
      s16x8 f[2][4];
#pragma unroll
      for (int i = 0; i <= 16; ++i) {
        if (i < 16) {
#pragma unroll
          for (int t = 0; t < 4; ++t) f[i & 1][t] = *(LAS s16x8*)(lds + i * 4096 + t * 1024 + l16);
        }
        if (i > 0) {
          const int i0 = i - 1;
#pragma unroll
          for (int t = 0; t < 4; ++t) acc[t] = mfma32(f[i0 & 1][t], as_frag(hf[i0]), acc[t]);
          if (i < 16) ILVX(4, 1, 0);
        }
        SCHED_FENCE();
        if (i < 16) WAIT_VM1(15 - i, hf[i]);
      }
    }
    TS(37);
    cnt_add(L_CNT);
    issue_w2(2, slot);
    asm volatile("" : "+v"(sv[0]), "+v"(sv[1]), "+v"(sv[2]), "+v"(sv[3]));
    float rr, mrr;
    {
      const float sp = (__uint_as_float(sv[0].x) + __uint_as_float(sv[1].x)) + (__uint_as_float(sv[2].x) + __uint_as_float(sv[3].x));
      const float qp = (__uint_as_float(sv[0].y) + __uint_as_float(sv[1].y)) + (__uint_as_float(sv[2].y) + __uint_as_float(sv[3].y));
      const float S = mfma4(1.f, sp, zero4)[0], Q = mfma4(1.f, qp, zero4)[0];
      const float mean = S * (1.f / 512.f);
      const float var = fmaxf(Q * (1.f / 512.f) - mean * mean, 0.f);
      const float r1 = __builtin_amdgcn_rsqf(var + 1e-5f);
      rr = r1 * __builtin_amdgcn_rsqf(var * r1 * r1 + 1e-5f);
      mrr = -mean * rr;
    }
    if (w < 4) __builtin_amdgcn_s_setprio(1);
    TS(38);
    auto epi = [&](int t) -> u32x2 {
      const f32x4 cs = *(LAS f32x4*)(lds + L_SP + t * 6144 + 4096 + g * 32), bv = *(LAS f32x4*)(lds + L_SP + t * 6144 + 4096 + g * 32 + 16);
      const f32x4 v = acc[t] * rr + (cs * mrr + bv);
      const f32x2 a = gelu2((f32x2){v[0], v[1]}), b = gelu2((f32x2){v[2], v[3]});
      return (u32x2){pk_bf16(a.x, a.y), pk_bf16(b.x, b.y)};
    };
    const unsigned dst = WS_HH + (unsigned)((tile3 * 8 + (C >> 1)) * 7168);
    const unsigned dstc = dst + (unsigned)((C & 1) * 3072) + l16;
    const unsigned foff = (unsigned)((tile3 * 16 + C) * 128);
    auto pass2 = [&](int i0) {
      s16x8 f[12];
#pragma unroll
      for (int i = 0; i < 12; ++i) f[i] = *(LAS s16x8*)(lds + L_G32 + i0 * 3072 + i * 1024 + l16);
#pragma unroll
      for (int i = 0; i < 4; ++i)
#pragma unroll
        for (int t = 0; t < 3; ++t) acc[4 + t] = mfma32(f[3 * i + t], as_frag(hf[i0 + i]), acc[4 + t]);
    };
    {
      pass2(0);
      const u32x2 e0 = epi(0);
      P2ILV();
      SCHED_FENCE();
      pass2(4);
      const u32x2 e1 = epi(1);
      P2ILV();
      st16_sc1(rs, dstc, (u32x4){e0.x, e0.y, e1.x, e1.y});
      SCHED_FENCE();
    }
    TS(39);
    {
      pass2(8);
      const u32x2 e2 = epi(2);
      P2ILV();
      SCHED_FENCE();
      pass2(12);
      const u32x2 e3 = epi(3);
      P2ILV();
      st16_sc1(rs, dstc + 1024, (u32x4){e2.x, e2.y, e3.x, e3.y});
      SCHED_FENCE();
    }
    cnt_add(L_CNT + 4);
    const u32x2 e4 = epi(4);
    SCHED_FENCE();
    WAIT_VM(0);
    if (lane == 0) st8_sc1(rs, WS_F4A + foff, (u32x2){tlo, thi});
    TS(10);
    read_w2(wr[2], slot);
    {
      const u32x2 e5 = epi(5);
      st16_sc1(rs, dstc + 2048, (u32x4){e4.x, e4.y, e5.x, e5.y});
      const u32x2 e6 = epi(6);
      st8_sc1(rs, dst + 6144 + l16 + (unsigned)((C & 1) * 8), e6);
    }
    WAIT_LGKM();
    issue_w2(3, slot);
    TS(40);
    WAIT_VM(4);
    if (lane == 0) st8_sc1(rs, WS_F4B + foff, (u32x2){tlo, thi});
    TS(11);
    __builtin_amdgcn_s_setprio(0);
    cnt_wait(L_CNT);
    issue_w2(4, (unsigned)(w * 8192));
    issue_w2(5, (unsigned)(w * 8192 + 4096));
    WAIT_VM(9);
    read_w2(wr[3], slot);
    WAIT_LGKM();
    issue_w2(6, slot);
  }
  TS(41);
  {
    const int tile4 = P * 8 + th * 4;
    const unsigned foff = (unsigned)(((tile4 + ((lane >> 1) & 3)) * 16 + 2 * w + (lane & 1)) * 128);
    const char* ab = ws + WS_HH + (unsigned)((tile4 * 8 + w) * 7168) + 4096;
    u32x4 af[7][4];
#define LD_A(n) do { _Pragma("unroll") for (int tt = 0; tt < 4; ++tt) GLD4S(af[n][tt], l16, ab + tt * 57344, ((n) < 6 ? ((n) & 1) * 3072 + ((n) >> 1) * 1024 : 6144) - 4096); } while (0)
#define MM_A(n, cnt) do { _Pragma("unroll") for (int h2 = 0; h2 < 2; ++h2) { WAIT_VM2((cnt) + 2 - 2 * h2, af[n][2 * h2], af[n][2 * h2 + 1]); \
      _Pragma("unroll") for (int tt = 2 * h2; tt < 2 * h2 + 2; ++tt) _Pragma("unroll") for (int t = 0; t < 4; ++t) acc[tt][t] = mfma32(wr[(n) & 3][t], as_frag(af[n][tt]), (n) == 0 ? zero4 : acc[tt][t]); \
      SCHED_FENCE(); } } while (0)
    wait_flags(ws, WS_F4A + foff, tlo, thi);
    TS(12);
    u32x2 fb;
    f32x4 acc[4][4];
    LD_A(0); LD_A(1);
    GLD2S(fb, foff, ws + WS_F4B, 0);
    LD_A(2); LD_A(3);
    MM_A(0, 13);
    MM_A(1, 9);
    WAIT_VM1(8, fb);
    if (__builtin_amdgcn_ballot_w64((fb.x == tlo) & (fb.y == thi)) != ~0ull) wait_flags(ws, WS_F4B + foff, tlo, thi);
    TS(42);
    LD_A(4); LD_A(5);
    read_w2(wr[0], (unsigned)(w * 8192));
    read_w2(wr[1], (unsigned)(w * 8192 + 4096));
    MM_A(2, 12);
    LD_A(6);
    read_w2(wr[2], slot);
    MM_A(3, 12);
    MM_A(4, 8);
    MM_A(5, 4);
    MM_A(6, 0);
    TS(44);
    cnt_wait(L_CNT + 4);
    f32x4 k0 = zero4, k1 = zero4;
#pragma unroll
    for (int d = 0; d < 8; ++d) {
      if (d == w) { k0 = acc[d >> 1][2 * (d & 1)]; k1 = acc[d >> 1][2 * (d & 1) + 1]; }
      else {
        const int i = (d - w - 1) & 7;
        LAS char* dst = lds + (i < 4 ? (unsigned)(w * 8192 + i * 2048) : i < 6 ? slot + (unsigned)((i - 4) * 2048) : L_G32 + (unsigned)(w * 6144)) + l16;
        *(LAS f32x4*)dst = acc[d >> 1][2 * (d & 1)];
        *(LAS f32x4*)(dst + 1024) = acc[d >> 1][2 * (d & 1) + 1];
      }
    }
    bar();
    TS(13);
#pragma unroll
    for (int i = 0; i < 7; ++i) {
      const int e = (w - 1 - i) & 7;
      LAS char* src = lds + (i < 4 ? (unsigned)(e * 8192 + i * 2048) : i < 6 ? L_SP + (unsigned)(e * 6144 + (i - 4) * 2048) : L_G32 + (unsigned)(e * 6144)) + l16;
      k0 += *(LAS f32x4*)src;
      k1 += *(LAS f32x4*)(src + 1024);
    }
    const f32x4 A = (g & 1) ? k1 : k0, B = (g & 1) ? k0 : k1;
    u32x4 o;
    o.x = pk_bf16(lo_f(hb.x) + A[0] + lo_f(bb2.x), hi_f(hb.x) + A[1] + hi_f(bb2.x));
    o.y = pk_bf16(lo_f(hb.y) + A[2] + lo_f(bb2.y), hi_f(hb.y) + A[3] + hi_f(bb2.y));
    o.z = pk_bf16(lo_f(hb.z) + B[0] + lo_f(bb2.z), hi_f(hb.z) + B[1] + hi_f(bb2.z));
    o.w = pk_bf16(lo_f(hb.w) + B[2] + lo_f(bb2.w), hi_f(hb.w) + B[3] + hi_f(bb2.w));
    const unsigned xo_off = (unsigned)(((tilef * 16 + r) * 512 + ct * 64 + 16 * g + 8 * khf) * 2);
    *(u32x4*)((char*)out + xo_off) = o;
  }
  TS(14);
}

extern "C" void kernel_launch(void* const* d_in, const int* in_sizes, int n_in, void* d_out, int out_size, void* d_ws, size_t ws_size, hipStream_t stream) {
  const bf16_t* x = (const bf16_t*)d_in[0]; const bf16_t* wqkv = (const bf16_t*)d_in[1]; const bf16_t* wo = (const bf16_t*)d_in[2];
  const bf16_t* w1 = (const bf16_t*)d_in[4]; const bf16_t* b1 = (const bf16_t*)d_in[5]; const bf16_t* w2 = (const bf16_t*)d_in[6]; const bf16_t* b2 = (const bf16_t*)d_in[7];
  (void)hipFuncSetAttribute((const void*)fused_block, hipFuncAttributeMaxDynamicSharedMemorySize, 163840);
  fused_block<<<256, 512, 163840, stream>>>(x, wqkv, wo, w1, b1, w2, b2, (char*)d_ws, (bf16_t*)d_out);
}
```

```cpp
#include <hip/hip_runtime.h>
#include <stdint.h>

typedef unsigned short bf16_t;
typedef unsigned long long u64;
typedef __attribute__((ext_vector_type(8))) short s16x8;
typedef __attribute__((ext_vector_type(4))) short s16x4;
typedef __attribute__((ext_vector_type(4))) float f32x4;
typedef __attribute__((ext_vector_type(2))) float f32x2;
typedef __attribute__((ext_vector_type(4))) unsigned u32x4;
typedef __attribute__((ext_vector_type(2))) unsigned u32x2;
#define LAS __attribute__((address_space(3)))
#define DI __device__ __forceinline__

constexpr unsigned WS_E    = 0;
constexpr unsigned WS_F1   = 65536;
constexpr unsigned WS_F2   = 262144;
constexpr unsigned WS_F3   = 524288;
constexpr unsigned WS_F4A  = 786432;
constexpr unsigned WS_F4B  = 1048576;
constexpr unsigned WS_KVP  = 1310720;
constexpr unsigned WS_O    = 2u << 20;
constexpr unsigned WS_H    = 4u << 20;
constexpr unsigned WS_HH   = 6u << 20;
constexpr unsigned WS_HS   = 14u << 20;
constexpr int KVP_STRIDE = 1152;

typedef __attribute__((ext_vector_type(2))) __bf16 bf16x2_t;
DI unsigned pk_bf16(float lo, float hi) { return __builtin_bit_cast(unsigned, __builtin_convertvector((f32x2){lo, hi}, bf16x2_t)); }
DI float lo_f(unsigned u) { return __uint_as_float(u << 16); }
DI float hi_f(unsigned u) { return __uint_as_float(u & 0xffff0000u); }

DI void glds16(const void* sbase, unsigned voff, unsigned lds_dst) {
  unsigned keep;
  asm volatile("s_mov_b32 %0, m0\n\ts_mov_b32 m0, %3\n\ts_nop 0\n\tglobal_load_lds_dwordx4 %1, %2\n\ts_mov_b32 m0, %0"
               : "=&s"(keep) : "v"(voff), "s"(sbase), "s"(lds_dst) : "memory");
}
#define GLD4(d, voff, sb, imm)  asm volatile("global_load_dwordx4 %0, %1, %2 offset:%3" : "=v"(d) : "v"(voff), "s"(sb), "i"(imm) : "memory")
#define GLD2(d, voff, sb, imm)  asm volatile("global_load_dwordx2 %0, %1, %2 offset:%3" : "=v"(d) : "v"(voff), "s"(sb), "i"(imm) : "memory")
#define GLD1(d, voff, sb, imm)  asm volatile("global_load_dword %0, %1, %2 offset:%3" : "=v"(d) : "v"(voff), "s"(sb), "i"(imm) : "memory")
#define GLD4S(d, voff, sb, imm) asm volatile("global_load_dwordx4 %0, %1, %2 offset:%3 sc1" : "=v"(d) : "v"(voff), "s"(sb), "i"(imm) : "memory")
#define GLD2S(d, voff, sb, imm) asm volatile("global_load_dwordx2 %0, %1, %2 offset:%3 sc1" : "=v"(d) : "v"(voff), "s"(sb), "i"(imm) : "memory")
#define GLD1S(d, voff, sb, imm) asm volatile("global_load_dword %0, %1, %2 offset:%3 sc1" : "=v"(d) : "v"(voff), "s"(sb), "i"(imm) : "memory")
#define WAIT_VM(n) asm volatile("s_waitcnt vmcnt(%0)" :: "i"(n) : "memory")
#define WAIT_VM1(n, a) asm volatile("s_waitcnt vmcnt(%1)" : "+v"(a) : "i"(n) : "memory")
#define WAIT_VM2(n, a, b) asm volatile("s_waitcnt vmcnt(%2)" : "+v"(a), "+v"(b) : "i"(n) : "memory")
#define WAIT_VM4(n, a, b, c, d) asm volatile("s_waitcnt vmcnt(%4)" : "+v"(a), "+v"(b), "+v"(c), "+v"(d) : "i"(n) : "memory")
#define WAIT_LGKM() asm volatile("s_waitcnt lgkmcnt(0)" ::: "memory")
#define MEMFENCE() asm volatile("" ::: "memory")
DI void bar() { asm volatile("s_waitcnt lgkmcnt(0)" ::: "memory"); __builtin_amdgcn_s_barrier(); asm volatile("" ::: "memory"); }
#define SCHED_FENCE() __builtin_amdgcn_sched_barrier(0)
#define RAW_BAR() do { asm volatile("" ::: "memory"); __builtin_amdgcn_s_barrier(); asm volatile("" ::: "memory"); } while (0)
#define ILVX(n, d, v) do { _Pragma("unroll") for (int z_ = 0; z_ < (n); ++z_) { __builtin_amdgcn_sched_group_barrier(0x008, 1, 0); if ((d) > 0) __builtin_amdgcn_sched_group_barrier(0x100, (d), 0); if ((v) > 0) __builtin_amdgcn_sched_group_barrier(0x002, (v), 0); } } while (0)

DI s16x8 tr_frag(LAS char* lds, unsigned a0, unsigned a1) {
  s16x4 a = __builtin_amdgcn_ds_read_tr16_b64_v4i16((LAS s16x4*)(lds + a0));
  s16x4 b = __builtin_amdgcn_ds_read_tr16_b64_v4i16((LAS s16x4*)(lds + a1));
  return __builtin_shufflevector(a, b, 0, 1, 2, 3, 4, 5, 6, 7);
}
DI f32x4 mfma32(s16x8 a, s16x8 b, f32x4 c) { return __builtin_amdgcn_mfma_f32_16x16x32_bf16(a, b, c, 0, 0, 0); }
DI f32x4 mfma16(s16x4 a, s16x4 b, f32x4 c) { return __builtin_amdgcn_mfma_f32_16x16x16bf16_1k(a, b, c, 0, 0, 0); }
DI f32x4 mfma4(float a, float b, f32x4 c) { return __builtin_amdgcn_mfma_f32_16x16x4f32(a, b, c, 0, 0, 0); }
DI float phi(float t) { return fmaxf(t, 0.f) + __builtin_amdgcn_fmed3f(__builtin_amdgcn_exp2f(t * 1.44269504089f), 0.f, 1.f) + 1e-6f; }
DI s16x8 as_frag(u32x4 v) { return __builtin_bit_cast(s16x8, v); }
DI u32x4 as_u4(s16x8 v) { return __builtin_bit_cast(u32x4, v); }
DI f32x4 as_f4(u32x4 v) { return __builtin_bit_cast(f32x4, v); }
DI float diag4(f32x4 d, int r) { const float t = (r & 1) ? d[1] : d[0], u = (r & 1) ? d[3] : d[2]; return (r & 2) ? u : t; }

typedef __amdgpu_buffer_rsrc_t rsrc_t;
DI void st16_sc1(rsrc_t rs, unsigned off, u32x4 v) { __builtin_amdgcn_raw_buffer_store_b128(v, rs, off, 0, 16); }
DI void st8_sc1(rsrc_t rs, unsigned off, u32x2 v) { __builtin_amdgcn_raw_buffer_store_b64(v, rs, off, 0, 16); }

DI void wait_flags(const char* ws, unsigned off, unsigned tlo, unsigned thi) {
#pragma nounroll
  for (int it = 0; it < (1 << 18); ++it) {
    u32x2 v;
    asm volatile("global_load_dwordx2 %0, %1, %2 sc1\n\ts_waitcnt vmcnt(0)" : "=v"(v) : "v"(off), "s"(ws) : "memory");
    const bool ok = (v.x == tlo) & (v.y == thi);
    if (__builtin_amdgcn_ballot_w64(ok) == ~0ull) break;
    __builtin_amdgcn_s_sleep(1);
  }
  asm volatile("" ::: "memory");
}

#define TS(i)

DI unsigned g64_rd(int g, int q, int pp, int u, int b) {
  int rho = 4 * g + q;
  return (unsigned)(128 * rho + 16 * ((2 * pp + u) ^ ((rho >> 1) & 3)) + 8 * (b ^ (pp & 1)));
}
DI f32x2 gelu2(f32x2 v) {
  const f32x2 t = v * v;
  const f32x2 a = v * (t * (f32x2){-0.1029432397f, -0.1029432397f} + (f32x2){-2.3022082f, -2.3022082f});
  const f32x2 d = (f32x2){__builtin_amdgcn_exp2f(a.x), __builtin_amdgcn_exp2f(a.y)} + (f32x2){1.f, 1.f};
  return v * (f32x2){__builtin_amdgcn_rcpf(d.x), __builtin_amdgcn_rcpf(d.y)};
}
DI int hcol(int cp, int j) {
  if (j < 64) return 64 * cp + j;
  const int sw = (cp & 3) == 1;
  return 1024 + 48 * cp + (j < 96 ? (j - 64) + (sw ? 16 : 0) : (j - 96) + (sw ? 0 : 32));
}
DI int w2_row(int e, int n, int rho) {
  const int gg = (rho >> 2) & 3, j = 4 * (rho >> 4) + (rho & 3);
  if (n < 6) { const int cp = 2 * e + (n & 1), u = n >> 1, jj = j ^ (4 * (gg & 1)); return u < 2 ? hcol(cp, 16 * gg + 8 * u + jj) : hcol(cp, 64) + 8 * gg + jj; }
  return hcol(2 * e + (j >> 2), 96) + 4 * gg + (j & 3);
}
#define CODE_LINES 0
#define QG_ISSUE 0
#define QG_FLAG 6
#define P2ILV()
#define USE_T1 1
#define TI 2
#define NT 2
#define T4A 1
#define T4B 1
#define QG_G64 8
#define CODE_PF 1
#define CODE_W 6
__global__ __launch_bounds__(512, 2) void fused_block(const bf16_t* __restrict__ x, const bf16_t* __restrict__ wqkv, const bf16_t* __restrict__ wo,
                                                      const bf16_t* __restrict__ w1, const bf16_t* __restrict__ b1, const bf16_t* __restrict__ w2,
                                                      const bf16_t* __restrict__ b2, char* ws, bf16_t* __restrict__ out) {
  extern __shared__ __attribute__((aligned(16))) char smem_[];
  LAS char* lds = (LAS char*)smem_;
  asm volatile("" :: "s"(x), "s"(wqkv), "s"(wo), "s"(w1), "s"(b1), "s"(w2), "s"(b2), "s"(ws), "s"(out));
  const int tid = threadIdx.x, lane = tid & 63;
  const int w = __builtin_amdgcn_readfirstlane(tid >> 6);
  const int bid = blockIdx.x;
  const int P = (bid & 7) * 2 + (bid >> 7), C = (bid >> 3) & 15, J = bid >> 3;
  const int g = lane >> 4, r = lane & 15, q = (lane >> 2) & 3, pp = lane & 3;
  const unsigned ldsb = (unsigned)(size_t)smem_;
  const rsrc_t rs = __builtin_amdgcn_make_buffer_rsrc(ws, 0, 0x7fffffff, 0x00020000);
  const unsigned l16 = (unsigned)lane * 16u;
  TS(0);
  const f32x4 zero4 = (f32x4){0, 0, 0, 0};
  const int ct = C & 7, th = C >> 3;
  constexpr unsigned L_WKV = 0, L_X = 65536, L_CS = 16384, L_T = 53248, L_G32 = 65536, L_SP = 114688;
  s16x8 ones; for (int j = 0; j < 8; ++j) ones[j] = (short)0x3f80;

  auto issue_w1p = [&](int kb, int pc) {
    auto srow = [&](int rho) { return 64 * (kb >> 1) + 16 * ((rho >> 2) & 3) + 8 * (kb & 1) + 4 * (rho >> 4) + (rho & 3); };
    if (pc < 4) { int rho = pc * 8 + (lane >> 3); glds16(w1, (unsigned)(srow(rho) * 3584 + C * 128 + (((lane & 7) ^ ((rho >> 1) & 3)) * 16)), ldsb + kb * 4096 + pc * 1024); }
    else if (pc < 6) { int rho = (pc - 4) * 16 + (lane >> 2); glds16(w1, (unsigned)(srow(rho) * 3584 + hcol(C, 64) * 2 + (((lane & 3) ^ ((rho >> 2) & 1)) * 16)), ldsb + L_G32 + kb * 3072 + (pc - 4) * 1024); }
    else { int rho = lane >> 1; glds16(w1, (unsigned)(srow(rho) * 3584 + hcol(C, 96) * 2 + (lane & 1) * 16), ldsb + L_G32 + kb * 3072 + 2048); }
  };
  unsigned rd4[4];
#pragma unroll
  for (int t = 0; t < 4; ++t) rd4[t] = g64_rd(g, q, pp, t >> 1, t & 1);
  auto lin64 = [&](int kb) {
    s16x8 f[4];
#pragma unroll
    for (int i = 0; i < 4; ++i) { const unsigned a = (unsigned)(kb * 4096) + rd4[i]; f[i] = tr_frag(lds, a, a + 2048); }
    WAIT_LGKM();
#pragma unroll
    for (int i = 0; i < 4; ++i) *(LAS s16x8*)(lds + kb * 4096 + i * 1024 + l16) = f[i];
  };
  const int m2 = w >> 2, kq = w & 3;

  u32x4 pq;
  unsigned tlo, thi;
  u32x4 xr = (u32x4){0, 0, 0, 0};
  const unsigned xs = L_X + (unsigned)(w * 12288);
  unsigned tw0 = 0, tw1 = 0;
  u32x4 ga[16], gz;
  const char* kb0 = ws + WS_KVP + (unsigned)((C * 2 + (w & 1)) * 16 * KVP_STRIDE);
  auto sweep_issue = [&]() {
    const unsigned zoff = (unsigned)(r * KVP_STRIDE + 1024 + g * 16);
#pragma unroll
    for (int i = 0; i < 16; ++i) GLD4S(ga[i], l16, kb0 + (i >> 1) * 2 * KVP_STRIDE, (i & 1) * KVP_STRIDE);
    GLD4S(gz, zoff, kb0, 0);
  };
  {
    u32x2 Ev = (u32x2){0u, 0u};
    u32x4 xf[16];
    const int xrow = lane >> 3;
    const unsigned xoff = (unsigned)(((P * 128 + w * 16 + xrow) * 512) * 2 + (((lane & 7) ^ (xrow & 7)) * 16));
    const int wk = lane >> 2;
    const unsigned wcol = (unsigned)((((((lane >> 1) & 1) ^ ((wk >> 3) & 1)) * 16) + (lane & 1) * 8) * 2);
    const unsigned woff = (unsigned)(wk * 1536 * 2) + wcol + (unsigned)(C * 64);
    auto issue_x = [&](int ch) {
      const unsigned dst = ldsb + xs + (ch % 6) * 2048;
      glds16(x, xoff + ch * 128, dst);
      glds16(x, xoff + ch * 128 + 8 * 1024, dst + 1024);
    };
    auto issue_wkv = [&](int ch) {
      int sub = w >> 1, seg = w & 1;
      glds16(wqkv, woff + (unsigned)((ch * 64 + sub * 16) * 3072 + (1 + seg) * 1024), ldsb + L_WKV + ch * 8192 + w * 1024);
    };
    auto issue_wq = [&](int i) {
      int blk = w * 4 + i;
      glds16(wqkv, woff + (unsigned)(blk * 16 * 3072), ldsb + xs + 4096 + i * 1024);
    };
    const int worow = lane >> 3;
    const unsigned wooff = (unsigned)(worow * 1024 + ct * 128 + (((lane & 7) ^ ((worow >> 1) & 3)) * 16));
    auto issue_wo = [&](int i) { int blk = w * 8 + i; glds16(wo, wooff + blk * 8192, ldsb + xs + (i < 4 ? 8192 + i * 1024 : (i - 4) * 1024)); };
    unsigned tc0 = 0, tc1 = 0;
    if (w == 7) asm volatile("global_load_dwordx2 %0, %1, %2 sc1" : "=v"(Ev) : "v"(WS_E), "s"(ws) : "memory");
    if (w == CODE_W) {
      u64 pc; unsigned len;
      asm volatile("s_getpc_b64 %0\n.Lfb_code0:\n\ts_mov_b32 %1, .Lfb_code1-.Lfb_code0" : "=s"(pc), "=s"(len));
      const unsigned co = (unsigned)(lane * 32 + J) * 128u;
      if (co < len) GLD1(Ev.x, co, pc, 0);
    }
#pragma unroll
    for (int ch = 0; ch < 8; ++ch) {
      issue_wkv(ch);
      if (ch < 6) issue_x(ch);
      if (ch == 1) {
        RAW_BAR();
        if (w < 6 && USE_T1) {
          if (lane < 32) GLD1(tc0, (unsigned)((w + 2) * 64 + 2 * J + g) * 3072u + 1024u + (unsigned)r * 128u, wqkv, 0);
          else if (lane < 40) GLD1(tc1, (unsigned)((bid & 7) * 256 + J * 8 + (lane - 32)) * 1024u + (unsigned)(w + 2) * 128u, x, 0);
        } else if (lane < 32) {
          const unsigned row = (unsigned)(J * 16 + (w & 1) * 8 + (lane >> 3));
          GLD1(tc0, row * 3072u + (unsigned)(lane & 7) * 128u, wqkv, 0);
          GLD1(tc1, (row + 4u) * 3072u + (unsigned)(lane & 7) * 128u, wqkv, 0);
        }
      }
      if (ch == 3) RAW_BAR();
    }
    TS(53);
    const unsigned wrd = (unsigned)((g >> 1) * 1024 + (8 * (g & 1) + q) * 64 + 8 * pp);
    const unsigned wrd0 = L_WKV + (g >> 1) * 1024 + wrd + 32u * (0 ^ (g & 1)), wrd1 = L_WKV + (g >> 1) * 1024 + wrd + 32u * (1 ^ (g & 1));
    const unsigned wrdc = ((w & 2) ? wrd1 : wrd0) + (unsigned)((w >> 2) * 4096 + (w & 1) * 1024);
    const unsigned xrd0 = xs + r * 128 + (unsigned)(((0 + g) ^ (r & 7)) * 16), xrd1 = xs + r * 128 + (unsigned)(((4 + g) ^ (r & 7)) * 16);
    const unsigned xrr = L_X + (unsigned)((4 * th + (w >> 1)) * 12288 + (ct % 6) * 2048 + r * 128 + (((2 * g + (w & 1)) ^ (r & 7)) * 16));

    s16x8 wf[2][2][4];
    s16x8 cf, cf7;
    f32x4 acc[4], cl = zero4, sa = zero4, qa = zero4;
    acc[0] = acc[1] = acc[2] = acc[3] = zero4;
#pragma unroll
    for (int ch = 0; ch <= 8; ++ch) {
      if (ch < 8) {
        if (ch == 2) WAIT_VM2(13, tc0, tc1);
        else WAIT_VM(ch == 0 ? 19 : ch == 1 ? 16 : ch == 3 ? 12 : ch == 4 ? 11 : ch == 5 ? 10 + NT : ch == 6 ? 6 + NT : 4 + NT);
        xf[2 * ch] = *(LAS u32x4*)(lds + xrd0 + (ch % 6) * 2048);
        xf[2 * ch + 1] = *(LAS u32x4*)(lds + xrd1 + (ch % 6) * 2048);
        bar();
        if (ch == 0) TS(45); if (ch == 2) TS(47); if (ch == 4) TS(49); if (ch == 6) TS(51); if (ch == 7) TS(52);
        if (ch == 1 || ch == 2) issue_x(5 + ch);
        else if (ch == 3 || ch == 4) { issue_wq(2 * (ch - 3)); issue_wq(2 * (ch - 3) + 1); }
        if (ch == 4 && NT > 0) {
          const unsigned idx = (unsigned)((J * 8 + w) * 16 + r), i8 = idx >> 3, lo = (idx & 7u) * 128u;
          const char* ta = g == 0 ? (const char*)wo + idx * 128u
                                  : (const char*)w2 + ((16u * (i8 >> 3) + (i8 & 7u)) * 1024u + lo);
          if (lane < (T4A ? 32 : 16)) asm volatile("global_load_dword %0, %1, off" : "=v"(tw0) : "v"(ta) : "memory");
          GLD1(tw1, (unsigned)((J * 8 + w) * 56 + (lane < 56 ? lane : 55)) * 128u, w1, 0);
        }
        if (ch == ct) xr = *(LAS u32x4*)(lds + xrr);
        SCHED_FENCE();
#pragma unroll
        for (int s = 0; s < 2; ++s)
#pragma unroll
          for (int t = 0; t < 4; ++t) {
            const unsigned a = ((t >> 1) ? wrd1 : wrd0) + ch * 8192 + s * 4096 + (t & 1) * 1024;
            wf[ch & 1][s][t] = tr_frag(lds, a, a + 256);
          }
      }
      if (ch > 0) {
        const int c0 = ch - 1;
        if (c0 < 6) cl = mfma32(ones, cf, cl);
#pragma unroll
        for (int s = 0; s < 2; ++s) {
          const s16x8 xa = as_frag(xf[c0 * 2 + s]);
#pragma unroll
          for (int t = 0; t < 4; ++t) acc[t] = mfma32(xa, wf[c0 & 1][s][t], acc[t]);
          sa = mfma32(ones, xa, sa);
          qa = mfma32(xa, xa, qa);
        }
        if (ch < 8) ILVX(8, 2, 0);
      }
      if (ch < 7) cf = tr_frag(lds, wrdc + ch * 8192, wrdc + ch * 8192 + 256);
      if (ch == 6) {
        cf7 = tr_frag(lds, wrdc + 7 * 8192, wrdc + 7 * 8192 + 256);
        cl = mfma32(ones, cf, cl);
        cl = mfma32(ones, cf7, cl);
        if (g == 0) *(LAS float*)(lds + L_CS + r * 32 + w * 4) = cl[0];
      }
      SCHED_FENCE();
    }
    TS(1);
    asm volatile("" : "+v"(Ev));
    if (w == 7 && lane == 0) *(LAS u32x2*)(lds + L_T + 3584) = Ev;
    const unsigned a0 = L_X + 4096 + wrd + 32u * (0 ^ (g & 1)), a1 = L_X + 4096 + wrd + 32u * (1 ^ (g & 1));
    float mean1, rstd1;
    {
      const float qd = (g == (r >> 2)) ? diag4(qa, r) : 0.f;
      const f32x4 Sr = mfma4(sa[0], 0.25f, zero4), Qr = mfma4(qd, 1.f, zero4);
      const f32x4 cs = *(LAS f32x4*)(lds + L_CS + r * 32) + *(LAS f32x4*)(lds + L_CS + r * 32 + 16);
      const float Qc = mfma4(1.f, qd, zero4)[0];
      float val[4][4];
#pragma unroll
      for (int j = 0; j < 4; ++j) {
        const float mu = Sr[j] * (1.f / 512.f); const float var = fmaxf(Qr[j] * (1.f / 512.f) - mu * mu, 0.f); const float rsd = __builtin_amdgcn_rsqf(var + 1e-5f);
#pragma unroll
        for (int t = 0; t < 4; ++t) { const float v = rsd * (acc[t][j] - mu * cs[t]); val[t][j] = (t & 1) ? v : phi(v); }
      }
      s16x4 one4; for (int j = 0; j < 4; ++j) one4[j] = (short)0x3f80;
#pragma unroll
      for (int hd = 0; hd < 2; ++hd) {
        const s16x4 pk4 = __builtin_bit_cast(s16x4, (u32x2){pk_bf16(val[2 * hd][0], val[2 * hd][1]), pk_bf16(val[2 * hd][2], val[2 * hd][3])});
        const s16x4 v4 = __builtin_bit_cast(s16x4, (u32x2){pk_bf16(val[2 * hd + 1][0], val[2 * hd + 1][1]), pk_bf16(val[2 * hd + 1][2], val[2 * hd + 1][3])});
        const f32x4 kvp = mfma16(pk4, v4, zero4), zp = mfma16(pk4, one4, zero4);
        *(LAS f32x4*)(lds + hd * 8192 + w * 1024 + l16) = kvp;
        if (r == 0) *(LAS f32x4*)(lds + L_T + 1024 + (hd * 8 + w) * 64 + g * 16) = zp;
      }
      mean1 = sa[0] * (1.f / 512.f); rstd1 = __builtin_amdgcn_rsqf(fmaxf(Qc * (1.f / 512.f) - mean1 * mean1, 0.f) + 1e-5f);
    }
    SCHED_FENCE();
    TS(22);
    bar();
    TS(24);
    {
      Ev = *(LAS u32x2*)(lds + L_T + 3584);
      const u64 E = ((u64)(unsigned)__builtin_amdgcn_readfirstlane(Ev.y) << 32) | (unsigned)__builtin_amdgcn_readfirstlane(Ev.x);
      const u64 T = E * 0x9E3779B97F4A7C15ull + 0xD1B54A32D192ED03ull;
      tlo = (unsigned)T; thi = (unsigned)(T >> 32);
    }
    if (w < 2) {
      f32x4 s = zero4, sz = zero4;
#pragma unroll
      for (int mm = 0; mm < 8; ++mm) {
        s += *(LAS f32x4*)(lds + w * 8192 + mm * 1024 + l16);
        sz += *(LAS f32x4*)(lds + L_T + 1024 + (w * 8 + mm) * 64 + (lane & 3) * 16);
      }
      const unsigned dst = WS_KVP + (unsigned)(((C * 2 + w) * 16 + P) * KVP_STRIDE) + l16;
      st16_sc1(rs, dst, (u32x4){pk_bf16(s[0], s[1]), tlo, pk_bf16(s[2], s[3]), tlo});
      if (lane < 4) st16_sc1(rs, dst + 1024, (u32x4){pk_bf16(sz[0], sz[1]), tlo, pk_bf16(sz[2], sz[3]), tlo});
      WAIT_VM(2 + NT);
    } else WAIT_VM(NT);
    {
      const unsigned o = (unsigned)(w * 12288);
      f32x4 c0 = mfma32(tr_frag(lds, a0 + o, a0 + o + 256), ones, zero4), c1 = mfma32(tr_frag(lds, a1 + o, a1 + o + 256), ones, zero4);
      c0 = mfma32(tr_frag(lds, a0 + o + 2048, a0 + o + 2304), ones, c0); c1 = mfma32(tr_frag(lds, a1 + o + 2048, a1 + o + 2304), ones, c1);
      if (r == 0) { *(LAS f32x4*)(lds + L_T + w * 128 + g * 32) = c0; *(LAS f32x4*)(lds + L_T + w * 128 + g * 32 + 16) = c1; }
    }
    bar();
    TS(23);
    f32x4 aq[2];
    aq[0] = aq[1] = zero4;
    {
      s16x8 f[2][4];
#pragma unroll
      for (int kk = 0; kk <= 8; ++kk) {
        if (kk < 8) {
#pragma unroll
          for (int s = 0; s < 2; ++s) {
            f[kk & 1][s * 2 + 0] = tr_frag(lds, a0 + kk * 12288 + s * 2048, a0 + kk * 12288 + s * 2048 + 256);
            f[kk & 1][s * 2 + 1] = tr_frag(lds, a1 + kk * 12288 + s * 2048, a1 + kk * 12288 + s * 2048 + 256);
          }
        }
        if (kk > 0) {
          const int k0 = kk - 1;
#pragma unroll
          for (int s = 0; s < 2; ++s) {
            aq[0] = mfma32(f[k0 & 1][s * 2 + 0], as_frag(xf[k0 * 2 + s]), aq[0]);
            aq[1] = mfma32(f[k0 & 1][s * 2 + 1], as_frag(xf[k0 * 2 + s]), aq[1]);
          }
          if (kk < 8) ILVX(4, 2, 0);
        }
        SCHED_FENCE();
        if (kk == QG_ISSUE) {
#pragma unroll
          for (int i = 0; i < 8; ++i) issue_wo(i);
          SCHED_FENCE();
        }
        if (kk == QG_G64) {
          WAIT_VM2(8, tw0, tw1);
          if ((w >> 1) == 1) sweep_issue();
          else {
#pragma unroll
            for (int i = 0; i < 4; ++i) issue_w1p(2 * w, i);
            if (w != 6) {
#pragma unroll
              for (int i = 0; i < 4; ++i) issue_w1p(2 * w + 1, i);
            }
          }
          SCHED_FENCE();
        }
      }
    }
    TS(25);
    {
      f32x4 c0 = zero4, c1 = zero4;
#pragma unroll
      for (int ww = 0; ww < 8; ++ww) { c0 += *(LAS f32x4*)(lds + L_T + ww * 128 + g * 32); c1 += *(LAS f32x4*)(lds + L_T + ww * 128 + g * 32 + 16); }
      float a[4], b[4];
#pragma unroll
      for (int j = 0; j < 4; ++j) { a[j] = phi(rstd1 * (aq[0][j] - mean1 * c0[j])); b[j] = phi(rstd1 * (aq[1][j] - mean1 * c1[j])); }
      pq.x = pk_bf16(a[0], a[1]); pq.y = pk_bf16(a[2], a[3]); pq.z = pk_bf16(b[0], b[1]); pq.w = pk_bf16(b[2], b[3]);
    }
    asm volatile("" : "+v"(pq), "+v"(xr));
  }
  TS(4);
  if ((w >> 1) == 1) {
    const int hd = w & 1;
    f32x4 s, z;
    auto sweep_check = [&]() -> bool {
      unsigned bad = 0;
      s = zero4;
#pragma unroll
      for (int i = 0; i < 16; i += 4) {
        WAIT_VM4(13 - i, ga[i], ga[i + 1], ga[i + 2], ga[i + 3]);
#pragma unroll
        for (int k = i; k < i + 4; ++k) {
          bad |= (ga[k].y ^ tlo) | (ga[k].w ^ tlo);
          s += (f32x4){lo_f(ga[k].x), hi_f(ga[k].x), lo_f(ga[k].z), hi_f(ga[k].z)};
        }
      }
      WAIT_VM1(0, gz);
      bad |= (gz.y ^ tlo) | (gz.w ^ tlo);
      z = (f32x4){lo_f(gz.x), hi_f(gz.x), lo_f(gz.z), hi_f(gz.z)};
      return __builtin_amdgcn_ballot_w64(bad != 0u) == 0ull;
    };
    if (!sweep_check()) {
#pragma nounroll
      for (int it = 0; it < (1 << 16); ++it) {
        TS(3);
        sweep_issue();
        if (sweep_check()) break;
      }
    }
#pragma unroll
    for (int i = 0; i < 8; ++i) issue_w1p(2 * w + (i >> 2), i & 3);
    TS(5);
    *(LAS u32x2*)(lds + L_T + 2048 + hd * 1024 + lane * 8) = (u32x2){pk_bf16(s[0], s[1]), pk_bf16(s[2], s[3])};
#pragma unroll
    for (int j = 0; j < 4; ++j) {
      float t = z[j];
      t += __builtin_bit_cast(float, __builtin_amdgcn_update_dpp(0, __builtin_bit_cast(int, t), 0x128, 0xf, 0xf, false));
      t += __builtin_bit_cast(float, __builtin_amdgcn_update_dpp(0, __builtin_bit_cast(int, t), 0x124, 0xf, 0xf, false));
      t += __builtin_bit_cast(float, __builtin_amdgcn_update_dpp(0, __builtin_bit_cast(int, t), 0x122, 0xf, 0xf, false));
      t += __builtin_bit_cast(float, __builtin_amdgcn_update_dpp(0, __builtin_bit_cast(int, t), 0x121, 0xf, 0xf, false));
      z[j] = t;
    }
    *(LAS u32x2*)(lds + L_T + 2048 + hd * 1024 + 512 + lane * 8) = (u32x2){pk_bf16(z[0], z[1]), pk_bf16(z[2], z[3])};
  } else {
    WAIT_VM(0);
    lin64(2 * w);
    if (w != 6) lin64(2 * w + 1);
  }
  TS(29);
  bar();
  {
    u32x4 o;
#pragma unroll
    for (int hd = 0; hd < 2; ++hd) {
      const s16x4 kvA = *(LAS s16x4*)(lds + L_T + 2048 + hd * 1024 + lane * 8);
      const s16x4 zA = *(LAS s16x4*)(lds + L_T + 2048 + hd * 1024 + 512 + lane * 8);
      u32x2 pu; pu.x = hd ? pq.z : pq.x; pu.y = hd ? pq.w : pq.y;
      const s16x4 p4 = __builtin_bit_cast(s16x4, pu);
      const f32x4 num = mfma16(kvA, p4, zero4);
      const f32x4 den = mfma16(zA, p4, zero4);
      const float inv = __builtin_amdgcn_rcpf(den[0] + 1e-6f);
      if (hd == 0) { o.x = pk_bf16(num[0] * inv, num[1] * inv); o.y = pk_bf16(num[2] * inv, num[3] * inv); }
      else { o.z = pk_bf16(num[0] * inv, num[1] * inv); o.w = pk_bf16(num[2] * inv, num[3] * inv); }
    }
    st16_sc1(rs, WS_O + (unsigned)(((P * 8 + w) * 16 + C) * 1024) + l16, o);
  }
  SCHED_FENCE();
  TS(30);
  s16x8 wof[4][4];
#pragma unroll
  for (int i = 0; i < 4; ++i)
#pragma unroll
    for (int t = 0; t < 4; ++t) { const unsigned a = L_X + (unsigned)((kq * 2 + (i >> 1)) * 12288 + ((i & 1) ? 0 : 8192)) + rd4[t]; wof[i][t] = tr_frag(lds, a, a + 2048); }
  SCHED_FENCE();
  WAIT_VM(0);
  if (lane == 0) st8_sc1(rs, WS_F2 + (unsigned)(((P * 8 + w) * 16 + C) * 128), (u32x2){tlo, thi});
  TS(6);
  bar();
  const int tile0 = P * 8 + th * 4 + 2 * m2;
  const int tilef = tile0 + (kq >> 1), khf = kq & 1;
  u32x4 bb2;
  u32x2 bb1;
  if ((w >> 1) == 1) { lin64(2 * w); lin64(2 * w + 1); }
  if (w == 6) {
#pragma unroll
    for (int i = 0; i < 4; ++i) issue_w1p(13, i);
  }
#pragma unroll
  for (int i = 0; i < 6; ++i) issue_w1p(2 * w + (i / 3), 4 + i % 3);
  {
    const unsigned b2o = (unsigned)((ct * 64 + 16 * g + 8 * khf) * 2);
    GLD4(bb2, b2o, b2, 0);
    const int wt = w < 7 ? w : 6;
    const int col = wt < 4 ? 16 * g + 8 * (wt >> 1) + 4 * ((wt & 1) ^ (g & 1)) : wt < 6 ? 64 + 8 * g + 4 * ((wt & 1) ^ (g & 1)) : 96 + 4 * g;
    const unsigned b1o = (unsigned)(hcol(C, col) * 2);
    GLD2(bb1, b1o, b1, 0);
  }
  TS(28);
  wait_flags(ws, WS_F2 + (unsigned)(((tile0 + ((lane >> 2) & 1)) * 16 + 4 * kq + (lane & 3)) * 128), tlo, thi);
  TS(7);
  asm volatile("" : "+v"(bb2), "+v"(bb1));
  u32x4 hb;
  {
    u32x4 of[2][4];
    const char* ob = ws + WS_O + (unsigned)((tile0 * 16 + kq * 4) * 1024);
#pragma unroll
    for (int i = 0; i < 4; ++i)
#pragma unroll
      for (int tt = 0; tt < 2; ++tt) GLD4S(of[tt][i], l16, ob + tt * 16384, i * 1024);
    f32x4 acc[2][4];
#pragma unroll
    for (int t = 0; t < 4; ++t) acc[0][t] = acc[1][t] = zero4;
#pragma unroll
    for (int i = 0; i < 4; ++i) {
      WAIT_VM2(6 - 2 * i, of[0][i], of[1][i]);
#pragma unroll
      for (int tt = 0; tt < 2; ++tt)
#pragma unroll
        for (int t = 0; t < 4; ++t) acc[tt][t] = mfma32(wof[i][t], as_frag(of[tt][i]), acc[tt][t]);
      SCHED_FENCE();
    }
    f32x4 k0 = zero4, k1 = zero4;
#pragma unroll
    for (int d = 0; d < 4; ++d) {
      if (d == kq) { k0 = acc[d >> 1][2 * (d & 1)]; k1 = acc[d >> 1][2 * (d & 1) + 1]; }
      else {
        LAS char* dst = lds + L_SP + ((m2 * 4 + d) * 3 + ((kq - d - 1) & 3)) * 2048 + l16;
        *(LAS f32x4*)dst = acc[d >> 1][2 * (d & 1)];
        *(LAS f32x4*)(dst + 1024) = acc[d >> 1][2 * (d & 1) + 1];
      }
    }
    TS(32);
    bar();
    TS(33);
#pragma unroll
    for (int s = 0; s < 3; ++s) {
      k0 += *(LAS f32x4*)(lds + L_SP + (w * 3 + s) * 2048 + l16);
      k1 += *(LAS f32x4*)(lds + L_SP + (w * 3 + s) * 2048 + 1024 + l16);
    }
    const f32x4 A = (g & 1) ? k1 : k0, B = (g & 1) ? k0 : k1;
    hb.x = pk_bf16(lo_f(xr.x) + A[0], hi_f(xr.x) + A[1]); hb.y = pk_bf16(lo_f(xr.y) + A[2], hi_f(xr.y) + A[3]);
    hb.z = pk_bf16(lo_f(xr.z) + B[0], hi_f(xr.z) + B[1]); hb.w = pk_bf16(lo_f(xr.w) + B[2], hi_f(xr.w) + B[3]);
    const unsigned blk = (unsigned)(tilef * 16 + 2 * ct + khf);
    st16_sc1(rs, WS_H + blk * 1024 + l16, hb);
    const f32x4 sS = mfma32(ones, as_frag(hb), zero4), sQ = mfma32(as_frag(hb), as_frag(hb), zero4);
    if (g == (r >> 2)) st8_sc1(rs, WS_HS + blk * 128 + (unsigned)r * 8u, (u32x2){__float_as_uint(sS[0]), __float_as_uint(diag4(sQ, r))});
  }
  TS(34);
  WAIT_VM(0);
  if (lane == 0) {
    st8_sc1(rs, WS_F3 + (unsigned)((tilef * 16 + 2 * ct + khf) * 128), (u32x2){tlo, thi});
    if (bid == 0 && w == 0) st8_sc1(rs, WS_E, (u32x2){tlo, thi});
  }
  TS(8);
  const unsigned slot = L_SP + (unsigned)(w * 6144);
  constexpr unsigned L_CNT = L_SP + 7 * 6144 + 4096;
  auto issue_w2 = [&](int n, unsigned dst) {
#pragma unroll
    for (int b = 0; b < 4; ++b) {
      const int rho = b * 8 + (lane >> 3);
      glds16(w2, (unsigned)(w2_row(w, n, rho) * 1024 + ct * 128 + (((lane & 7) ^ ((rho >> 1) & 3)) * 16)), ldsb + dst + b * 1024);
    }
  };
  s16x8 wr[4][4];
  auto read_w2 = [&](s16x8* f, unsigned src) {
#pragma unroll
    for (int t = 0; t < 4; ++t) f[t] = tr_frag(lds, src + rd4[t], src + rd4[t] + 2048);
  };
  auto cnt_add = [&](unsigned a) { if (lane == 0) __hip_atomic_fetch_add((LAS unsigned*)(lds + a), 1u, __ATOMIC_RELAXED, __HIP_MEMORY_SCOPE_WORKGROUP); MEMFENCE(); };
  auto cnt_wait = [&](unsigned a) {
#pragma nounroll
    for (int it = 0; it < (1 << 20); ++it) {
      MEMFENCE();
      const unsigned v = *(volatile LAS unsigned*)(lds + a);
      if (__builtin_amdgcn_readfirstlane(v) >= 8u) break;
      __builtin_amdgcn_s_sleep(1);
    }
    MEMFENCE();
  };
  issue_w2(0, slot);
  unsigned tw3 = 0;
  {
    const unsigned k = (unsigned)(40 * J + 5 * w) + (unsigned)((lane < 40 ? lane : 39) >> 3);
    GLD1(tw3, (k < 512u ? 16u * (k >> 3) + 8u + (k & 7u) : k + 512u) * 1024u + (unsigned)(lane & 7) * 128u, w2, 0);
  }
  {
    const int rho0 = 4 * g + q;
    s16x8 f[6];
#pragma unroll
    for (int i = 0; i < 2; ++i) {
      const unsigned base = L_G32 + (unsigned)((2 * w + i) * 3072);
#pragma unroll
      for (int b = 0; b < 2; ++b) { const unsigned a = base + (unsigned)(64 * rho0 + 16 * (pp ^ ((rho0 >> 2) & 1)) + 8 * (b ^ (pp & 1))); f[i * 3 + b] = tr_frag(lds, a, a + 1024); }
      const unsigned a = base + 2048u + (unsigned)(32 * rho0 + 8 * pp);
      f[i * 3 + 2] = tr_frag(lds, a, a + 512);
    }
    WAIT_LGKM();
#pragma unroll
    for (int i = 0; i < 6; ++i) *(LAS s16x8*)(lds + L_G32 + w * 6144 + i * 1024 + l16) = f[i];
  }
  if (w == 6) lin64(13);
  bar();
  TS(35);
  if (w < 7) {
    const unsigned a0 = (w < 4 ? (unsigned)(w * 1024) : L_G32 + (unsigned)((w - 4) * 1024)) + l16, st = w < 4 ? 4096u : 3072u;
    f32x4 c0 = zero4, c1 = zero4;
#pragma unroll
    for (int i = 0; i < 16; i += 2) {
      c0 = mfma32(*(LAS s16x8*)(lds + a0 + i * st), ones, c0);
      c1 = mfma32(*(LAS s16x8*)(lds + a0 + (i + 1) * st), ones, c1);
    }
    if (r == 0) {
      *(LAS f32x4*)(lds + slot + 4096 + g * 32) = c0 + c1;
      *(LAS f32x4*)(lds + slot + 4096 + g * 32 + 16) = (f32x4){lo_f(bb1.x), hi_f(bb1.x), lo_f(bb1.y), hi_f(bb1.y)};
    }
  } else if (lane == 0) *(LAS u32x2*)(lds + L_CNT) = (u32x2){0u, 0u};
  WAIT_VM(T4B);
  read_w2(wr[0], slot);
  bar();
  issue_w2(1, slot);
  TS(36);
  const int tile3 = P * 8 + w;
  wait_flags(ws, WS_F3 + (unsigned)((tile3 * 16 + r) * 128), tlo, thi);
  TS(9);
  asm volatile("" : "+v"(tw3));
  {
    u32x4 hf[16];
    u32x2 sv[4];
    const char* hbp = ws + WS_H + (unsigned)(tile3 * 16384);
    {
      const char* sp = ws + WS_HS + (unsigned)(tile3 * 2048);
      const unsigned l8 = (unsigned)lane * 8u;
#pragma unroll
      for (int i = 0; i < 4; ++i) GLD2S(sv[i], l8, sp, i * 512);
    }
#pragma unroll
    for (int i = 0; i < 16; ++i) GLD4S(hf[i], l16, hbp + (i >> 2) * 4096, (i & 3) * 1024);
    read_w2(wr[1], slot);
    if (w < 4) __builtin_amdgcn_s_setprio(3); else __builtin_amdgcn_s_setprio(2);
    f32x4 acc[7];
#pragma unroll
    for (int t = 0; t < 7; ++t) acc[t] = zero4;
    {
      s16x8 f[2][4];
#pragma unroll
      for (int i = 0; i <= 16; ++i) {
        if (i < 16) {
#pragma unroll
          for (int t = 0; t < 4; ++t) f[i & 1][t] = *(LAS s16x8*)(lds + i * 4096 + t * 1024 + l16);
        }
        if (i > 0) {
          const int i0 = i - 1;
#pragma unroll
          for (int t = 0; t < 4; ++t) acc[t] = mfma32(f[i0 & 1][t], as_frag(hf[i0]), acc[t]);
          if (i < 16) ILVX(4, 1, 0);
        }
        SCHED_FENCE();
        if (i < 16) WAIT_VM1(15 - i, hf[i]);
      }
    }
    TS(37);
    cnt_add(L_CNT);
    issue_w2(2, slot);
    asm volatile("" : "+v"(sv[0]), "+v"(sv[1]), "+v"(sv[2]), "+v"(sv[3]));
    float rr, mrr;
    {
      const float sp = (__uint_as_float(sv[0].x) + __uint_as_float(sv[1].x)) + (__uint_as_float(sv[2].x) + __uint_as_float(sv[3].x));
      const float qp = (__uint_as_float(sv[0].y) + __uint_as_float(sv[1].y)) + (__uint_as_float(sv[2].y) + __uint_as_float(sv[3].y));
      const float S = mfma4(1.f, sp, zero4)[0], Q = mfma4(1.f, qp, zero4)[0];
      const float mean = S * (1.f / 512.f);
      const float var = fmaxf(Q * (1.f / 512.f) - mean * mean, 0.f);
      const float r1 = __builtin_amdgcn_rsqf(var + 1e-5f);
      rr = r1 * __builtin_amdgcn_rsqf(var * r1 * r1 + 1e-5f);
      mrr = -mean * rr;
    }
    if (w < 4) __builtin_amdgcn_s_setprio(1);
    TS(38);
    auto epi = [&](int t) -> u32x2 {
      const f32x4 cs = *(LAS f32x4*)(lds + L_SP + t * 6144 + 4096 + g * 32), bv = *(LAS f32x4*)(lds + L_SP + t * 6144 + 4096 + g * 32 + 16);
      const f32x4 v = acc[t] * rr + (cs * mrr + bv);
      const f32x2 a = gelu2((f32x2){v[0], v[1]}), b = gelu2((f32x2){v[2], v[3]});
      return (u32x2){pk_bf16(a.x, a.y), pk_bf16(b.x, b.y)};
    };
    const unsigned dst = WS_HH + (unsigned)((tile3 * 8 + (C >> 1)) * 7168);
    const unsigned dstc = dst + (unsigned)((C & 1) * 3072) + l16;
    const unsigned foff = (unsigned)((tile3 * 16 + C) * 128);
    auto pass2 = [&](int i0) {
      s16x8 f[12];
#pragma unroll
      for (int i = 0; i < 12; ++i) f[i] = *(LAS s16x8*)(lds + L_G32 + i0 * 3072 + i * 1024 + l16);
#pragma unroll
      for (int i = 0; i < 4; ++i)
#pragma unroll
        for (int t = 0; t < 3; ++t) acc[4 + t] = mfma32(f[3 * i + t], as_frag(hf[i0 + i]), acc[4 + t]);
    };
    {
      pass2(0);
      const u32x2 e0 = epi(0);
      P2ILV();
      SCHED_FENCE();
      pass2(4);
      const u32x2 e1 = epi(1);
      P2ILV();
      st16_sc1(rs, dstc, (u32x4){e0.x, e0.y, e1.x, e1.y});
      SCHED_FENCE();
    }
    TS(39);
    {
      pass2(8);
      const u32x2 e2 = epi(2);
      P2ILV();
      SCHED_FENCE();
      pass2(12);
      const u32x2 e3 = epi(3);
      P2ILV();
      st16_sc1(rs, dstc + 1024, (u32x4){e2.x, e2.y, e3.x, e3.y});
      SCHED_FENCE();
    }
    cnt_add(L_CNT + 4);
    const u32x2 e4 = epi(4);
    SCHED_FENCE();
    WAIT_VM(0);
    if (lane == 0) st8_sc1(rs, WS_F4A + foff, (u32x2){tlo, thi});
    TS(10);
    read_w2(wr[2], slot);
    {
      const u32x2 e5 = epi(5);
      st16_sc1(rs, dstc + 2048, (u32x4){e4.x, e4.y, e5.x, e5.y});
      const u32x2 e6 = epi(6);
      st8_sc1(rs, dst + 6144 + l16 + (unsigned)((C & 1) * 8), e6);
    }
    WAIT_LGKM();
    issue_w2(3, slot);
    TS(40);
    WAIT_VM(4);
    if (lane == 0) st8_sc1(rs, WS_F4B + foff, (u32x2){tlo, thi});
    TS(11);
    __builtin_amdgcn_s_setprio(0);
    cnt_wait(L_CNT);
    issue_w2(4, (unsigned)(w * 8192));
    issue_w2(5, (unsigned)(w * 8192 + 4096));
    WAIT_VM(9);
    read_w2(wr[3], slot);
    WAIT_LGKM();
    issue_w2(6, slot);
  }
  TS(41);
  {
    const int tile4 = P * 8 + th * 4;
    const unsigned foff = (unsigned)(((tile4 + ((lane >> 1) & 3)) * 16 + 2 * w + (lane & 1)) * 128);
    const char* ab = ws + WS_HH + (unsigned)((tile4 * 8 + w) * 7168) + 4096;
    u32x4 af[7][4];
#define LD_A(n) do { _Pragma("unroll") for (int tt = 0; tt < 4; ++tt) GLD4S(af[n][tt], l16, ab + tt * 57344, ((n) < 6 ? ((n) & 1) * 3072 + ((n) >> 1) * 1024 : 6144) - 4096); } while (0)
#define MM_A(n, cnt) do { _Pragma("unroll") for (int h2 = 0; h2 < 2; ++h2) { WAIT_VM2((cnt) + 2 - 2 * h2, af[n][2 * h2], af[n][2 * h2 + 1]); \
      _Pragma("unroll") for (int tt = 2 * h2; tt < 2 * h2 + 2; ++tt) _Pragma("unroll") for (int t = 0; t < 4; ++t) acc[tt][t] = mfma32(wr[(n) & 3][t], as_frag(af[n][tt]), (n) == 0 ? zero4 : acc[tt][t]); \
      SCHED_FENCE(); } } while (0)
    wait_flags(ws, WS_F4A + foff, tlo, thi);
    TS(12);
    u32x2 fb;
    f32x4 acc[4][4];
    LD_A(0); LD_A(1);
    GLD2S(fb, foff, ws + WS_F4B, 0);
    LD_A(2); LD_A(3);
    MM_A(0, 13);
    MM_A(1, 9);
    WAIT_VM1(8, fb);
    if (__builtin_amdgcn_ballot_w64((fb.x == tlo) & (fb.y == thi)) != ~0ull) wait_flags(ws, WS_F4B + foff, tlo, thi);
    TS(42);
    LD_A(4); LD_A(5);
    read_w2(wr[0], (unsigned)(w * 8192));
    read_w2(wr[1], (unsigned)(w * 8192 + 4096));
    MM_A(2, 12);
    LD_A(6);
    read_w2(wr[2], slot);
    MM_A(3, 12);
    MM_A(4, 8);
    MM_A(5, 4);
    MM_A(6, 0);
    TS(44);
    cnt_wait(L_CNT + 4);
    f32x4 k0 = zero4, k1 = zero4;
#pragma unroll
    for (int d = 0; d < 8; ++d) {
      if (d == w) { k0 = acc[d >> 1][2 * (d & 1)]; k1 = acc[d >> 1][2 * (d & 1) + 1]; }
      else {
        const int i = (d - w - 1) & 7;
        LAS char* dst = lds + (i < 4 ? (unsigned)(w * 8192 + i * 2048) : i < 6 ? slot + (unsigned)((i - 4) * 2048) : L_G32 + (unsigned)(w * 6144)) + l16;
        *(LAS f32x4*)dst = acc[d >> 1][2 * (d & 1)];
        *(LAS f32x4*)(dst + 1024) = acc[d >> 1][2 * (d & 1) + 1];
      }
    }
    bar();
    TS(13);
#pragma unroll
    for (int i = 0; i < 7; ++i) {
      const int e = (w - 1 - i) & 7;
      LAS char* src = lds + (i < 4 ? (unsigned)(e * 8192 + i * 2048) : i < 6 ? L_SP + (unsigned)(e * 6144 + (i - 4) * 2048) : L_G32 + (unsigned)(e * 6144)) + l16;
      k0 += *(LAS f32x4*)src;
      k1 += *(LAS f32x4*)(src + 1024);
    }
    const f32x4 A = (g & 1) ? k1 : k0, B = (g & 1) ? k0 : k1;
    u32x4 o;
    o.x = pk_bf16(lo_f(hb.x) + A[0] + lo_f(bb2.x), hi_f(hb.x) + A[1] + hi_f(bb2.x));
    o.y = pk_bf16(lo_f(hb.y) + A[2] + lo_f(bb2.y), hi_f(hb.y) + A[3] + hi_f(bb2.y));
    o.z = pk_bf16(lo_f(hb.z) + B[0] + lo_f(bb2.z), hi_f(hb.z) + B[1] + hi_f(bb2.z));
    o.w = pk_bf16(lo_f(hb.w) + B[2] + lo_f(bb2.w), hi_f(hb.w) + B[3] + hi_f(bb2.w));
    const unsigned xo_off = (unsigned)(((tilef * 16 + r) * 512 + ct * 64 + 16 * g + 8 * khf) * 2);
    *(u32x4*)((char*)out + xo_off) = o;
  }
  TS(14);
  asm volatile(".Lfb_code1:");
}

extern "C" void kernel_launch(void* const* d_in, const int* in_sizes, int n_in, void* d_out, int out_size, void* d_ws, size_t ws_size, hipStream_t stream) {
  const bf16_t* x = (const bf16_t*)d_in[0]; const bf16_t* wqkv = (const bf16_t*)d_in[1]; const bf16_t* wo = (const bf16_t*)d_in[2];
  const bf16_t* w1 = (const bf16_t*)d_in[4]; const bf16_t* b1 = (const bf16_t*)d_in[5]; const bf16_t* w2 = (const bf16_t*)d_in[6]; const bf16_t* b2 = (const bf16_t*)d_in[7];
  (void)hipFuncSetAttribute((const void*)fused_block, hipFuncAttributeMaxDynamicSharedMemorySize, 163840);
  fused_block<<<256, 512, 163840, stream>>>(x, wqkv, wo, w1, b1, w2, b2, (char*)d_ws, (bf16_t*)d_out);
}
```

```cpp
#include <hip/hip_runtime.h>
#include <stdint.h>

typedef unsigned short bf16_t;
typedef unsigned long long u64;
typedef __attribute__((ext_vector_type(8))) short s16x8;
typedef __attribute__((ext_vector_type(4))) short s16x4;
typedef __attribute__((ext_vector_type(4))) float f32x4;
typedef __attribute__((ext_vector_type(2))) float f32x2;
typedef __attribute__((ext_vector_type(4))) unsigned u32x4;
typedef __attribute__((ext_vector_type(2))) unsigned u32x2;
#define LAS __attribute__((address_space(3)))
#define DI __device__ __forceinline__

constexpr unsigned WS_E    = 0;
constexpr unsigned WS_F1   = 65536;
constexpr unsigned WS_F2   = 262144;
constexpr unsigned WS_F3   = 524288;
constexpr unsigned WS_F4A  = 786432;
constexpr unsigned WS_F4B  = 1048576;
constexpr unsigned WS_KVP  = 1310720;
constexpr unsigned WS_O    = 2u << 20;
constexpr unsigned WS_H    = 4u << 20;
constexpr unsigned WS_HH   = 6u << 20;
constexpr unsigned WS_HS   = 14u << 20;
constexpr int KVP_STRIDE = 1152;

typedef __attribute__((ext_vector_type(2))) __bf16 bf16x2_t;
DI unsigned pk_bf16(float lo, float hi) { return __builtin_bit_cast(unsigned, __builtin_convertvector((f32x2){lo, hi}, bf16x2_t)); }
DI float lo_f(unsigned u) { return __uint_as_float(u << 16); }
DI float hi_f(unsigned u) { return __uint_as_float(u & 0xffff0000u); }

DI void glds16(const void* sbase, unsigned voff, unsigned lds_dst) {
  unsigned keep;
  asm volatile("s_mov_b32 %0, m0\n\ts_mov_b32 m0, %3\n\ts_nop 0\n\tglobal_load_lds_dwordx4 %1, %2\n\ts_mov_b32 m0, %0"
               : "=&s"(keep) : "v"(voff), "s"(sbase), "s"(lds_dst) : "memory");
}
#define GLD4(d, voff, sb, imm)  asm volatile("global_load_dwordx4 %0, %1, %2 offset:%3" : "=v"(d) : "v"(voff), "s"(sb), "i"(imm) : "memory")
#define GLD2(d, voff, sb, imm)  asm volatile("global_load_dwordx2 %0, %1, %2 offset:%3" : "=v"(d) : "v"(voff), "s"(sb), "i"(imm) : "memory")
#define GLD1(d, voff, sb, imm)  asm volatile("global_load_dword %0, %1, %2 offset:%3" : "=v"(d) : "v"(voff), "s"(sb), "i"(imm) : "memory")
#define GLD4S(d, voff, sb, imm) asm volatile("global_load_dwordx4 %0, %1, %2 offset:%3 sc1" : "=v"(d) : "v"(voff), "s"(sb), "i"(imm) : "memory")
#define GLD2S(d, voff, sb, imm) asm volatile("global_load_dwordx2 %0, %1, %2 offset:%3 sc1" : "=v"(d) : "v"(voff), "s"(sb), "i"(imm) : "memory")
#define GLD1S(d, voff, sb, imm) asm volatile("global_load_dword %0, %1, %2 offset:%3 sc1" : "=v"(d) : "v"(voff), "s"(sb), "i"(imm) : "memory")
#define WAIT_VM(n) asm volatile("s_waitcnt vmcnt(%0)" :: "i"(n) : "memory")
#define WAIT_VM1(n, a) asm volatile("s_waitcnt vmcnt(%1)" : "+v"(a) : "i"(n) : "memory")
#define WAIT_VM2(n, a, b) asm volatile("s_waitcnt vmcnt(%2)" : "+v"(a), "+v"(b) : "i"(n) : "memory")
#define WAIT_VM4(n, a, b, c, d) asm volatile("s_waitcnt vmcnt(%4)" : "+v"(a), "+v"(b), "+v"(c), "+v"(d) : "i"(n) : "memory")
#define WAIT_LGKM() asm volatile("s_waitcnt lgkmcnt(0)" ::: "memory")
#define MEMFENCE() asm volatile("" ::: "memory")
DI void bar() { asm volatile("s_waitcnt lgkmcnt(0)" ::: "memory"); __builtin_amdgcn_s_barrier(); asm volatile("" ::: "memory"); }
#define SCHED_FENCE() __builtin_amdgcn_sched_barrier(0)
#define RAW_BAR() do { asm volatile("" ::: "memory"); __builtin_amdgcn_s_barrier(); asm volatile("" ::: "memory"); } while (0)
#define ILVX(n, d, v) do { _Pragma("unroll") for (int z_ = 0; z_ < (n); ++z_) { __builtin_amdgcn_sched_group_barrier(0x008, 1, 0); if ((d) > 0) __builtin_amdgcn_sched_group_barrier(0x100, (d), 0); if ((v) > 0) __builtin_amdgcn_sched_group_barrier(0x002, (v), 0); } } while (0)

DI s16x8 tr_frag(LAS char* lds, unsigned a0, unsigned a1) {
  s16x4 a = __builtin_amdgcn_ds_read_tr16_b64_v4i16((LAS s16x4*)(lds + a0));
  s16x4 b = __builtin_amdgcn_ds_read_tr16_b64_v4i16((LAS s16x4*)(lds + a1));
  return __builtin_shufflevector(a, b, 0, 1, 2, 3, 4, 5, 6, 7);
}
DI f32x4 mfma32(s16x8 a, s16x8 b, f32x4 c) { return __builtin_amdgcn_mfma_f32_16x16x32_bf16(a, b, c, 0, 0, 0); }
DI f32x4 mfma16(s16x4 a, s16x4 b, f32x4 c) { return __builtin_amdgcn_mfma_f32_16x16x16bf16_1k(a, b, c, 0, 0, 0); }
DI f32x4 mfma4(float a, float b, f32x4 c) { return __builtin_amdgcn_mfma_f32_16x16x4f32(a, b, c, 0, 0, 0); }
DI float phi(float t) { return fmaxf(t, 0.f) + __builtin_amdgcn_fmed3f(__builtin_amdgcn_exp2f(t * 1.44269504089f), 0.f, 1.f) + 1e-6f; }
DI s16x8 as_frag(u32x4 v) { return __builtin_bit_cast(s16x8, v); }
DI u32x4 as_u4(s16x8 v) { return __builtin_bit_cast(u32x4, v); }
DI f32x4 as_f4(u32x4 v) { return __builtin_bit_cast(f32x4, v); }
DI float diag4(f32x4 d, int r) { const float t = (r & 1) ? d[1] : d[0], u = (r & 1) ? d[3] : d[2]; return (r & 2) ? u : t; }

typedef __amdgpu_buffer_rsrc_t rsrc_t;
DI void st16_sc1(rsrc_t rs, unsigned off, u32x4 v) { __builtin_amdgcn_raw_buffer_store_b128(v, rs, off, 0, 16); }
DI void st8_sc1(rsrc_t rs, unsigned off, u32x2 v) { __builtin_amdgcn_raw_buffer_store_b64(v, rs, off, 0, 16); }

DI void wait_flags(const char* ws, unsigned off, unsigned tlo, unsigned thi) {
#pragma nounroll
  for (int it = 0; it < (1 << 18); ++it) {
    u32x2 v;
    asm volatile("global_load_dwordx2 %0, %1, %2 sc1\n\ts_waitcnt vmcnt(0)" : "=v"(v) : "v"(off), "s"(ws) : "memory");
    const bool ok = (v.x == tlo) & (v.y == thi);
    if (__builtin_amdgcn_ballot_w64(ok) == ~0ull) break;
    __builtin_amdgcn_s_sleep(1);
  }
  asm volatile("" ::: "memory");
}

#define TS(i)

DI unsigned g64_rd(int g, int q, int pp, int u, int b) {
  int rho = 4 * g + q;
  return (unsigned)(128 * rho + 16 * ((2 * pp + u) ^ ((rho >> 1) & 3)) + 8 * (b ^ (pp & 1)));
}
DI f32x2 gelu2(f32x2 v) {
  const f32x2 t = v * v;
  const f32x2 a = v * (t * (f32x2){-0.1029432397f, -0.1029432397f} + (f32x2){-2.3022082f, -2.3022082f});
  const f32x2 d = (f32x2){__builtin_amdgcn_exp2f(a.x), __builtin_amdgcn_exp2f(a.y)} + (f32x2){1.f, 1.f};
  return v * (f32x2){__builtin_amdgcn_rcpf(d.x), __builtin_amdgcn_rcpf(d.y)};
}
DI int hcol(int cp, int j) {
  if (j < 64) return 64 * cp + j;
  const int sw = (cp & 3) == 1;
  return 1024 + 48 * cp + (j < 96 ? (j - 64) + (sw ? 16 : 0) : (j - 96) + (sw ? 0 : 32));
}
DI int w2_row(int e, int n, int rho) {
  const int gg = (rho >> 2) & 3, j = 4 * (rho >> 4) + (rho & 3);
  if (n < 6) { const int cp = 2 * e + (n & 1), u = n >> 1, jj = j ^ (4 * (gg & 1)); return u < 2 ? hcol(cp, 16 * gg + 8 * u + jj) : hcol(cp, 64) + 8 * gg + jj; }
  return hcol(2 * e + (j >> 2), 96) + 4 * gg + (j & 3);
}
#define CODE_LINES 0
#define QG_ISSUE 0
#define QG_FLAG 6
#define P2ILV()
#define USE_T1 1
#define TI 2
#define NT 2
#define T4A 1
#define T4B 1
#define QG_G64 8
#define CODE_PF 1
#define CODE_W 6
#define OUT_AUX 0
__global__ __launch_bounds__(512, 2) void fused_block(const bf16_t* __restrict__ x, const bf16_t* __restrict__ wqkv, const bf16_t* __restrict__ wo,
                                                      const bf16_t* __restrict__ w1, const bf16_t* __restrict__ b1, const bf16_t* __restrict__ w2,
                                                      const bf16_t* __restrict__ b2, char* ws, bf16_t* __restrict__ out) {
  extern __shared__ __attribute__((aligned(16))) char smem_[];
  LAS char* lds = (LAS char*)smem_;
  asm volatile("" :: "s"(x), "s"(wqkv), "s"(wo), "s"(w1), "s"(b1), "s"(w2), "s"(b2), "s"(ws), "s"(out));
  const int tid = threadIdx.x, lane = tid & 63;
  const int w = __builtin_amdgcn_readfirstlane(tid >> 6);
  const int bid = blockIdx.x;
  const int P = (bid & 7) * 2 + (bid >> 7), C = (bid >> 3) & 15, J = bid >> 3;
  const int g = lane >> 4, r = lane & 15, q = (lane >> 2) & 3, pp = lane & 3;
  const unsigned ldsb = (unsigned)(size_t)smem_;
  const rsrc_t rs = __builtin_amdgcn_make_buffer_rsrc(ws, 0, 0x7fffffff, 0x00020000);
  const unsigned l16 = (unsigned)lane * 16u;
  TS(0);
  const f32x4 zero4 = (f32x4){0, 0, 0, 0};
  const int ct = C & 7, th = C >> 3;
  constexpr unsigned L_WKV = 0, L_X = 65536, L_CS = 16384, L_T = 53248, L_G32 = 65536, L_SP = 114688;
  s16x8 ones; for (int j = 0; j < 8; ++j) ones[j] = (short)0x3f80;

  auto issue_w1p = [&](int kb, int pc) {
    auto srow = [&](int rho) { return 64 * (kb >> 1) + 16 * ((rho >> 2) & 3) + 8 * (kb & 1) + 4 * (rho >> 4) + (rho & 3); };
    if (pc < 4) { int rho = pc * 8 + (lane >> 3); glds16(w1, (unsigned)(srow(rho) * 3584 + C * 128 + (((lane & 7) ^ ((rho >> 1) & 3)) * 16)), ldsb + kb * 4096 + pc * 1024); }
    else if (pc < 6) { int rho = (pc - 4) * 16 + (lane >> 2); glds16(w1, (unsigned)(srow(rho) * 3584 + hcol(C, 64) * 2 + (((lane & 3) ^ ((rho >> 2) & 1)) * 16)), ldsb + L_G32 + kb * 3072 + (pc - 4) * 1024); }
    else { int rho = lane >> 1; glds16(w1, (unsigned)(srow(rho) * 3584 + hcol(C, 96) * 2 + (lane & 1) * 16), ldsb + L_G32 + kb * 3072 + 2048); }
  };
  unsigned rd4[4];
#pragma unroll
  for (int t = 0; t < 4; ++t) rd4[t] = g64_rd(g, q, pp, t >> 1, t & 1);
  auto lin64 = [&](int kb) {
    s16x8 f[4];
#pragma unroll
    for (int i = 0; i < 4; ++i) { const unsigned a = (unsigned)(kb * 4096) + rd4[i]; f[i] = tr_frag(lds, a, a + 2048); }
    WAIT_LGKM();
#pragma unroll
    for (int i = 0; i < 4; ++i) *(LAS s16x8*)(lds + kb * 4096 + i * 1024 + l16) = f[i];
  };
  const int m2 = w >> 2, kq = w & 3;

  u32x4 pq;
  unsigned tlo, thi;
  u32x4 xr = (u32x4){0, 0, 0, 0};
  const unsigned xs = L_X + (unsigned)(w * 12288);
  unsigned tw0 = 0, tw1 = 0;
  u32x4 ga[16], gz;
  const char* kb0 = ws + WS_KVP + (unsigned)((C * 2 + (w & 1)) * 16 * KVP_STRIDE);
  auto sweep_issue = [&]() {
    const unsigned zoff = (unsigned)(r * KVP_STRIDE + 1024 + g * 16);
#pragma unroll
    for (int i = 0; i < 16; ++i) GLD4S(ga[i], l16, kb0 + (i >> 1) * 2 * KVP_STRIDE, (i & 1) * KVP_STRIDE);
    GLD4S(gz, zoff, kb0, 0);
  };
  {
    u32x2 Ev = (u32x2){0u, 0u};
    u32x4 xf[16];
    const int xrow = lane >> 3;
    const unsigned xoff = (unsigned)(((P * 128 + w * 16 + xrow) * 512) * 2 + (((lane & 7) ^ (xrow & 7)) * 16));
    const int wk = lane >> 2;
    const unsigned wcol = (unsigned)((((((lane >> 1) & 1) ^ ((wk >> 3) & 1)) * 16) + (lane & 1) * 8) * 2);
    const unsigned woff = (unsigned)(wk * 1536 * 2) + wcol + (unsigned)(C * 64);
    auto issue_x = [&](int ch) {
      const unsigned dst = ldsb + xs + (ch % 6) * 2048;
      glds16(x, xoff + ch * 128, dst);
      glds16(x, xoff + ch * 128 + 8 * 1024, dst + 1024);
    };
    auto issue_wkv = [&](int ch) {
      int sub = w >> 1, seg = w & 1;
      glds16(wqkv, woff + (unsigned)((ch * 64 + sub * 16) * 3072 + (1 + seg) * 1024), ldsb + L_WKV + ch * 8192 + w * 1024);
    };
    auto issue_wq = [&](int i) {
      int blk = w * 4 + i;
      glds16(wqkv, woff + (unsigned)(blk * 16 * 3072), ldsb + xs + 4096 + i * 1024);
    };
    const int worow = lane >> 3;
    const unsigned wooff = (unsigned)(worow * 1024 + ct * 128 + (((lane & 7) ^ ((worow >> 1) & 3)) * 16));
    auto issue_wo = [&](int i) { int blk = w * 8 + i; glds16(wo, wooff + blk * 8192, ldsb + xs + (i < 4 ? 8192 + i * 1024 : (i - 4) * 1024)); };
    unsigned tc0 = 0, tc1 = 0;
    if (w == 7) {
      const char* ea = lane < 15 ? (const char*)ws + (unsigned)lane * 1048576u
                     : lane < 17 ? (const char*)out + (unsigned)(lane - 15) * 1048576u
                     : lane == 17 ? (const char*)b1 : lane == 18 ? (const char*)b2 : (const char*)ws;
      asm volatile("global_load_dwordx2 %0, %1, off sc1" : "=v"(Ev) : "v"(ea) : "memory");
    }
    if (w == CODE_W) {
      u64 pc; unsigned len;
      asm volatile("s_getpc_b64 %0\n.Lfb_code0:\n\ts_mov_b32 %1, .Lfb_code1-.Lfb_code0" : "=s"(pc), "=s"(len));
      const unsigned co = (unsigned)(lane * 32 + J) * 128u;
      if (co < len) GLD1(Ev.x, co, pc, 0);
    }
#pragma unroll
    for (int ch = 0; ch < 8; ++ch) {
      issue_wkv(ch);
      if (ch < 6) issue_x(ch);
      if (ch == 1) {
        RAW_BAR();
        if (w < 6 && USE_T1) {
          if (lane < 32) GLD1(tc0, (unsigned)((w + 2) * 64 + 2 * J + g) * 3072u + 1024u + (unsigned)r * 128u, wqkv, 0);
          else if (lane < 40) GLD1(tc1, (unsigned)((bid & 7) * 256 + J * 8 + (lane - 32)) * 1024u + (unsigned)(w + 2) * 128u, x, 0);
        } else if (lane < 32) {
          const unsigned row = (unsigned)(J * 16 + (w & 1) * 8 + (lane >> 3));
          GLD1(tc0, row * 3072u + (unsigned)(lane & 7) * 128u, wqkv, 0);
          GLD1(tc1, (row + 4u) * 3072u + (unsigned)(lane & 7) * 128u, wqkv, 0);
        }
      }
      if (ch == 3) RAW_BAR();
    }
    TS(53);
    const unsigned wrd = (unsigned)((g >> 1) * 1024 + (8 * (g & 1) + q) * 64 + 8 * pp);
    const unsigned wrd0 = L_WKV + (g >> 1) * 1024 + wrd + 32u * (0 ^ (g & 1)), wrd1 = L_WKV + (g >> 1) * 1024 + wrd + 32u * (1 ^ (g & 1));
    const unsigned wrdc = ((w & 2) ? wrd1 : wrd0) + (unsigned)((w >> 2) * 4096 + (w & 1) * 1024);
    const unsigned xrd0 = xs + r * 128 + (unsigned)(((0 + g) ^ (r & 7)) * 16), xrd1 = xs + r * 128 + (unsigned)(((4 + g) ^ (r & 7)) * 16);
    const unsigned xrr = L_X + (unsigned)((4 * th + (w >> 1)) * 12288 + (ct % 6) * 2048 + r * 128 + (((2 * g + (w & 1)) ^ (r & 7)) * 16));

    s16x8 wf[2][2][4];
    s16x8 cf, cf7;
    f32x4 acc[4], cl = zero4, sa = zero4, qa = zero4;
    acc[0] = acc[1] = acc[2] = acc[3] = zero4;
#pragma unroll
    for (int ch = 0; ch <= 8; ++ch) {
      if (ch < 8) {
        if (ch == 2) WAIT_VM2(13, tc0, tc1);
        else WAIT_VM(ch == 0 ? 19 : ch == 1 ? 16 : ch == 3 ? 12 : ch == 4 ? 11 : ch == 5 ? 10 + NT : ch == 6 ? 6 + NT : 4 + NT);
        xf[2 * ch] = *(LAS u32x4*)(lds + xrd0 + (ch % 6) * 2048);
        xf[2 * ch + 1] = *(LAS u32x4*)(lds + xrd1 + (ch % 6) * 2048);
        bar();
        if (ch == 0) TS(45); if (ch == 2) TS(47); if (ch == 4) TS(49); if (ch == 6) TS(51); if (ch == 7) TS(52);
        if (ch == 1 || ch == 2) issue_x(5 + ch);
        else if (ch == 3 || ch == 4) { issue_wq(2 * (ch - 3)); issue_wq(2 * (ch - 3) + 1); }
        if (ch == 4 && NT > 0) {
          const unsigned idx = (unsigned)((J * 8 + w) * 16 + r), i8 = idx >> 3, lo = (idx & 7u) * 128u;
          const char* ta = g == 0 ? (const char*)wo + idx * 128u
                                  : (const char*)w2 + ((16u * (i8 >> 3) + (i8 & 7u)) * 1024u + lo);
          if (lane < (T4A ? 32 : 16)) asm volatile("global_load_dword %0, %1, off" : "=v"(tw0) : "v"(ta) : "memory");
          GLD1(tw1, (unsigned)((J * 8 + w) * 56 + (lane < 56 ? lane : 55)) * 128u, w1, 0);
        }
        if (ch == ct) xr = *(LAS u32x4*)(lds + xrr);
        SCHED_FENCE();
#pragma unroll
        for (int s = 0; s < 2; ++s)
#pragma unroll
          for (int t = 0; t < 4; ++t) {
            const unsigned a = ((t >> 1) ? wrd1 : wrd0) + ch * 8192 + s * 4096 + (t & 1) * 1024;
            wf[ch & 1][s][t] = tr_frag(lds, a, a + 256);
          }
      }
      if (ch > 0) {
        const int c0 = ch - 1;
        if (c0 < 6) cl = mfma32(ones, cf, cl);
#pragma unroll
        for (int s = 0; s < 2; ++s) {
          const s16x8 xa = as_frag(xf[c0 * 2 + s]);
#pragma unroll
          for (int t = 0; t < 4; ++t) acc[t] = mfma32(xa, wf[c0 & 1][s][t], acc[t]);
          sa = mfma32(ones, xa, sa);
          qa = mfma32(xa, xa, qa);
        }
        if (ch < 8) ILVX(8, 2, 0);
      }
      if (ch < 7) cf = tr_frag(lds, wrdc + ch * 8192, wrdc + ch * 8192 + 256);
      if (ch == 6) {
        cf7 = tr_frag(lds, wrdc + 7 * 8192, wrdc + 7 * 8192 + 256);
        cl = mfma32(ones, cf, cl);
        cl = mfma32(ones, cf7, cl);
        if (g == 0) *(LAS float*)(lds + L_CS + r * 32 + w * 4) = cl[0];
      }
      SCHED_FENCE();
    }
    TS(1);
    asm volatile("" : "+v"(Ev));
    if (w == 7 && lane == 0) *(LAS u32x2*)(lds + L_T + 3584) = Ev;
    const unsigned a0 = L_X + 4096 + wrd + 32u * (0 ^ (g & 1)), a1 = L_X + 4096 + wrd + 32u * (1 ^ (g & 1));
    float mean1, rstd1;
    {
      const float qd = (g == (r >> 2)) ? diag4(qa, r) : 0.f;
      const f32x4 Sr = mfma4(sa[0], 0.25f, zero4), Qr = mfma4(qd, 1.f, zero4);
      const f32x4 cs = *(LAS f32x4*)(lds + L_CS + r * 32) + *(LAS f32x4*)(lds + L_CS + r * 32 + 16);
      const float Qc = mfma4(1.f, qd, zero4)[0];
      float val[4][4];
#pragma unroll
      for (int j = 0; j < 4; ++j) {
        const float mu = Sr[j] * (1.f / 512.f); const float var = fmaxf(Qr[j] * (1.f / 512.f) - mu * mu, 0.f); const float rsd = __builtin_amdgcn_rsqf(var + 1e-5f);
#pragma unroll
        for (int t = 0; t < 4; ++t) { const float v = rsd * (acc[t][j] - mu * cs[t]); val[t][j] = (t & 1) ? v : phi(v); }
      }
      s16x4 one4; for (int j = 0; j < 4; ++j) one4[j] = (short)0x3f80;
#pragma unroll
      for (int hd = 0; hd < 2; ++hd) {
        const s16x4 pk4 = __builtin_bit_cast(s16x4, (u32x2){pk_bf16(val[2 * hd][0], val[2 * hd][1]), pk_bf16(val[2 * hd][2], val[2 * hd][3])});
        const s16x4 v4 = __builtin_bit_cast(s16x4, (u32x2){pk_bf16(val[2 * hd + 1][0], val[2 * hd + 1][1]), pk_bf16(val[2 * hd + 1][2], val[2 * hd + 1][3])});
        const f32x4 kvp = mfma16(pk4, v4, zero4), zp = mfma16(pk4, one4, zero4);
        *(LAS f32x4*)(lds + hd * 8192 + w * 1024 + l16) = kvp;
        if (r == 0) *(LAS f32x4*)(lds + L_T + 1024 + (hd * 8 + w) * 64 + g * 16) = zp;
      }
      mean1 = sa[0] * (1.f / 512.f); rstd1 = __builtin_amdgcn_rsqf(fmaxf(Qc * (1.f / 512.f) - mean1 * mean1, 0.f) + 1e-5f);
    }
    SCHED_FENCE();
    TS(22);
    bar();
    TS(24);
    {
      Ev = *(LAS u32x2*)(lds + L_T + 3584);
      const u64 E = ((u64)(unsigned)__builtin_amdgcn_readfirstlane(Ev.y) << 32) | (unsigned)__builtin_amdgcn_readfirstlane(Ev.x);
      const u64 T = E * 0x9E3779B97F4A7C15ull + 0xD1B54A32D192ED03ull;
      tlo = (unsigned)T; thi = (unsigned)(T >> 32);
    }
    if (w < 2) {
      f32x4 s = zero4, sz = zero4;
#pragma unroll
      for (int mm = 0; mm < 8; ++mm) {
        s += *(LAS f32x4*)(lds + w * 8192 + mm * 1024 + l16);
        sz += *(LAS f32x4*)(lds + L_T + 1024 + (w * 8 + mm) * 64 + (lane & 3) * 16);
      }
      const unsigned dst = WS_KVP + (unsigned)(((C * 2 + w) * 16 + P) * KVP_STRIDE) + l16;
      st16_sc1(rs, dst, (u32x4){pk_bf16(s[0], s[1]), tlo, pk_bf16(s[2], s[3]), tlo});
      if (lane < 4) st16_sc1(rs, dst + 1024, (u32x4){pk_bf16(sz[0], sz[1]), tlo, pk_bf16(sz[2], sz[3]), tlo});
      WAIT_VM(2 + NT);
    } else WAIT_VM(NT);
    {
      const unsigned o = (unsigned)(w * 12288);
      f32x4 c0 = mfma32(tr_frag(lds, a0 + o, a0 + o + 256), ones, zero4), c1 = mfma32(tr_frag(lds, a1 + o, a1 + o + 256), ones, zero4);
      c0 = mfma32(tr_frag(lds, a0 + o + 2048, a0 + o + 2304), ones, c0); c1 = mfma32(tr_frag(lds, a1 + o + 2048, a1 + o + 2304), ones, c1);
      if (r == 0) { *(LAS f32x4*)(lds + L_T + w * 128 + g * 32) = c0; *(LAS f32x4*)(lds + L_T + w * 128 + g * 32 + 16) = c1; }
    }
    bar();
    TS(23);
    f32x4 aq[2];
    aq[0] = aq[1] = zero4;
    {
      s16x8 f[2][4];
#pragma unroll
      for (int kk = 0; kk <= 8; ++kk) {
        if (kk < 8) {
#pragma unroll
          for (int s = 0; s < 2; ++s) {
            f[kk & 1][s * 2 + 0] = tr_frag(lds, a0 + kk * 12288 + s * 2048, a0 + kk * 12288 + s * 2048 + 256);
            f[kk & 1][s * 2 + 1] = tr_frag(lds, a1 + kk * 12288 + s * 2048, a1 + kk * 12288 + s * 2048 + 256);
          }
        }
        if (kk > 0) {
          const int k0 = kk - 1;
#pragma unroll
          for (int s = 0; s < 2; ++s) {
            aq[0] = mfma32(f[k0 & 1][s * 2 + 0], as_frag(xf[k0 * 2 + s]), aq[0]);
            aq[1] = mfma32(f[k0 & 1][s * 2 + 1], as_frag(xf[k0 * 2 + s]), aq[1]);
          }
          if (kk < 8) ILVX(4, 2, 0);
        }
        SCHED_FENCE();
        if (kk == QG_ISSUE) {
#pragma unroll
          for (int i = 0; i < 8; ++i) issue_wo(i);
          SCHED_FENCE();
        }
        if (kk == QG_G64) {
          WAIT_VM2(8, tw0, tw1);
          if ((w >> 1) == 1) sweep_issue();
          else {
#pragma unroll
            for (int i = 0; i < 4; ++i) issue_w1p(2 * w, i);
            if (w != 6) {
#pragma unroll
              for (int i = 0; i < 4; ++i) issue_w1p(2 * w + 1, i);
            }
          }
          SCHED_FENCE();
        }
      }
    }
    TS(25);
    {
      f32x4 c0 = zero4, c1 = zero4;
#pragma unroll
      for (int ww = 0; ww < 8; ++ww) { c0 += *(LAS f32x4*)(lds + L_T + ww * 128 + g * 32); c1 += *(LAS f32x4*)(lds + L_T + ww * 128 + g * 32 + 16); }
      float a[4], b[4];
#pragma unroll
      for (int j = 0; j < 4; ++j) { a[j] = phi(rstd1 * (aq[0][j] - mean1 * c0[j])); b[j] = phi(rstd1 * (aq[1][j] - mean1 * c1[j])); }
      pq.x = pk_bf16(a[0], a[1]); pq.y = pk_bf16(a[2], a[3]); pq.z = pk_bf16(b[0], b[1]); pq.w = pk_bf16(b[2], b[3]);
    }
    asm volatile("" : "+v"(pq), "+v"(xr));
  }
  TS(4);
  if ((w >> 1) == 1) {
    const int hd = w & 1;
    f32x4 s, z;
    auto sweep_check = [&]() -> bool {
      unsigned bad = 0;
      s = zero4;
#pragma unroll
      for (int i = 0; i < 16; i += 4) {
        WAIT_VM4(13 - i, ga[i], ga[i + 1], ga[i + 2], ga[i + 3]);
#pragma unroll
        for (int k = i; k < i + 4; ++k) {
          bad |= (ga[k].y ^ tlo) | (ga[k].w ^ tlo);
          s += (f32x4){lo_f(ga[k].x), hi_f(ga[k].x), lo_f(ga[k].z), hi_f(ga[k].z)};
        }
      }
      WAIT_VM1(0, gz);
      bad |= (gz.y ^ tlo) | (gz.w ^ tlo);
      z = (f32x4){lo_f(gz.x), hi_f(gz.x), lo_f(gz.z), hi_f(gz.z)};
      return __builtin_amdgcn_ballot_w64(bad != 0u) == 0ull;
    };
    if (!sweep_check()) {
#pragma nounroll
      for (int it = 0; it < (1 << 16); ++it) {
        TS(3);
        sweep_issue();
        if (sweep_check()) break;
      }
    }
#pragma unroll
    for (int i = 0; i < 8; ++i) issue_w1p(2 * w + (i >> 2), i & 3);
    TS(5);
    *(LAS u32x2*)(lds + L_T + 2048 + hd * 1024 + lane * 8) = (u32x2){pk_bf16(s[0], s[1]), pk_bf16(s[2], s[3])};
#pragma unroll
    for (int j = 0; j < 4; ++j) {
      float t = z[j];
      t += __builtin_bit_cast(float, __builtin_amdgcn_update_dpp(0, __builtin_bit_cast(int, t), 0x128, 0xf, 0xf, false));
      t += __builtin_bit_cast(float, __builtin_amdgcn_update_dpp(0, __builtin_bit_cast(int, t), 0x124, 0xf, 0xf, false));
      t += __builtin_bit_cast(float, __builtin_amdgcn_update_dpp(0, __builtin_bit_cast(int, t), 0x122, 0xf, 0xf, false));
      t += __builtin_bit_cast(float, __builtin_amdgcn_update_dpp(0, __builtin_bit_cast(int, t), 0x121, 0xf, 0xf, false));
      z[j] = t;
    }
    *(LAS u32x2*)(lds + L_T + 2048 + hd * 1024 + 512 + lane * 8) = (u32x2){pk_bf16(z[0], z[1]), pk_bf16(z[2], z[3])};
  } else {
    WAIT_VM(0);
    lin64(2 * w);
    if (w != 6) lin64(2 * w + 1);
  }
  TS(29);
  bar();
  {
    u32x4 o;
#pragma unroll
    for (int hd = 0; hd < 2; ++hd) {
      const s16x4 kvA = *(LAS s16x4*)(lds + L_T + 2048 + hd * 1024 + lane * 8);
      const s16x4 zA = *(LAS s16x4*)(lds + L_T + 2048 + hd * 1024 + 512 + lane * 8);
      u32x2 pu; pu.x = hd ? pq.z : pq.x; pu.y = hd ? pq.w : pq.y;
      const s16x4 p4 = __builtin_bit_cast(s16x4, pu);
      const f32x4 num = mfma16(kvA, p4, zero4);
      const f32x4 den = mfma16(zA, p4, zero4);
      const float inv = __builtin_amdgcn_rcpf(den[0] + 1e-6f);
      if (hd == 0) { o.x = pk_bf16(num[0] * inv, num[1] * inv); o.y = pk_bf16(num[2] * inv, num[3] * inv); }
      else { o.z = pk_bf16(num[0] * inv, num[1] * inv); o.w = pk_bf16(num[2] * inv, num[3] * inv); }
    }
    st16_sc1(rs, WS_O + (unsigned)(((P * 8 + w) * 16 + C) * 1024) + l16, o);
  }
  SCHED_FENCE();
  TS(30);
  s16x8 wof[4][4];
#pragma unroll
  for (int i = 0; i < 4; ++i)
#pragma unroll
    for (int t = 0; t < 4; ++t) { const unsigned a = L_X + (unsigned)((kq * 2 + (i >> 1)) * 12288 + ((i & 1) ? 0 : 8192)) + rd4[t]; wof[i][t] = tr_frag(lds, a, a + 2048); }
  SCHED_FENCE();
  WAIT_VM(0);
  if (lane == 0) st8_sc1(rs, WS_F2 + (unsigned)(((P * 8 + w) * 16 + C) * 128), (u32x2){tlo, thi});
  TS(6);
  bar();
  const int tile0 = P * 8 + th * 4 + 2 * m2;
  const int tilef = tile0 + (kq >> 1), khf = kq & 1;
  u32x4 bb2;
  u32x2 bb1;
  if ((w >> 1) == 1) { lin64(2 * w); lin64(2 * w + 1); }
  if (w == 6) {
#pragma unroll
    for (int i = 0; i < 4; ++i) issue_w1p(13, i);
  }
#pragma unroll
  for (int i = 0; i < 6; ++i) issue_w1p(2 * w + (i / 3), 4 + i % 3);
  {
    const unsigned b2o = (unsigned)((ct * 64 + 16 * g + 8 * khf) * 2);
    GLD4(bb2, b2o, b2, 0);
    const int wt = w < 7 ? w : 6;
    const int col = wt < 4 ? 16 * g + 8 * (wt >> 1) + 4 * ((wt & 1) ^ (g & 1)) : wt < 6 ? 64 + 8 * g + 4 * ((wt & 1) ^ (g & 1)) : 96 + 4 * g;
    const unsigned b1o = (unsigned)(hcol(C, col) * 2);
    GLD2(bb1, b1o, b1, 0);
  }
  TS(28);
  wait_flags(ws, WS_F2 + (unsigned)(((tile0 + ((lane >> 2) & 1)) * 16 + 4 * kq + (lane & 3)) * 128), tlo, thi);
  TS(7);
  asm volatile("" : "+v"(bb2), "+v"(bb1));
  u32x4 hb;
  {
    u32x4 of[2][4];
    const char* ob = ws + WS_O + (unsigned)((tile0 * 16 + kq * 4) * 1024);
#pragma unroll
    for (int i = 0; i < 4; ++i)
#pragma unroll
      for (int tt = 0; tt < 2; ++tt) GLD4S(of[tt][i], l16, ob + tt * 16384, i * 1024);
    f32x4 acc[2][4];
#pragma unroll
    for (int t = 0; t < 4; ++t) acc[0][t] = acc[1][t] = zero4;
#pragma unroll
    for (int i = 0; i < 4; ++i) {
      WAIT_VM2(6 - 2 * i, of[0][i], of[1][i]);
#pragma unroll
      for (int tt = 0; tt < 2; ++tt)
#pragma unroll
        for (int t = 0; t < 4; ++t) acc[tt][t] = mfma32(wof[i][t], as_frag(of[tt][i]), acc[tt][t]);
      SCHED_FENCE();
    }
    f32x4 k0 = zero4, k1 = zero4;
#pragma unroll
    for (int d = 0; d < 4; ++d) {
      if (d == kq) { k0 = acc[d >> 1][2 * (d & 1)]; k1 = acc[d >> 1][2 * (d & 1) + 1]; }
      else {
        LAS char* dst = lds + L_SP + ((m2 * 4 + d) * 3 + ((kq - d - 1) & 3)) * 2048 + l16;
        *(LAS f32x4*)dst = acc[d >> 1][2 * (d & 1)];
        *(LAS f32x4*)(dst + 1024) = acc[d >> 1][2 * (d & 1) + 1];
      }
    }
    TS(32);
    bar();
    TS(33);
#pragma unroll
    for (int s = 0; s < 3; ++s) {
      k0 += *(LAS f32x4*)(lds + L_SP + (w * 3 + s) * 2048 + l16);
      k1 += *(LAS f32x4*)(lds + L_SP + (w * 3 + s) * 2048 + 1024 + l16);
    }
    const f32x4 A = (g & 1) ? k1 : k0, B = (g & 1) ? k0 : k1;
    hb.x = pk_bf16(lo_f(xr.x) + A[0], hi_f(xr.x) + A[1]); hb.y = pk_bf16(lo_f(xr.y) + A[2], hi_f(xr.y) + A[3]);
    hb.z = pk_bf16(lo_f(xr.z) + B[0], hi_f(xr.z) + B[1]); hb.w = pk_bf16(lo_f(xr.w) + B[2], hi_f(xr.w) + B[3]);
    const unsigned blk = (unsigned)(tilef * 16 + 2 * ct + khf);
    st16_sc1(rs, WS_H + blk * 1024 + l16, hb);
    const f32x4 sS = mfma32(ones, as_frag(hb), zero4), sQ = mfma32(as_frag(hb), as_frag(hb), zero4);
    if (g == (r >> 2)) st8_sc1(rs, WS_HS + blk * 128 + (unsigned)r * 8u, (u32x2){__float_as_uint(sS[0]), __float_as_uint(diag4(sQ, r))});
  }
  TS(34);
  WAIT_VM(0);
  if (lane == 0) {
    st8_sc1(rs, WS_F3 + (unsigned)((tilef * 16 + 2 * ct + khf) * 128), (u32x2){tlo, thi});
    if (bid == 0 && w == 0) st8_sc1(rs, WS_E, (u32x2){tlo, thi});
  }
  TS(8);
  const unsigned slot = L_SP + (unsigned)(w * 6144);
  constexpr unsigned L_CNT = L_SP + 7 * 6144 + 4096;
  auto issue_w2 = [&](int n, unsigned dst) {
#pragma unroll
    for (int b = 0; b < 4; ++b) {
      const int rho = b * 8 + (lane >> 3);
      glds16(w2, (unsigned)(w2_row(w, n, rho) * 1024 + ct * 128 + (((lane & 7) ^ ((rho >> 1) & 3)) * 16)), ldsb + dst + b * 1024);
    }
  };
  s16x8 wr[4][4];
  auto read_w2 = [&](s16x8* f, unsigned src) {
#pragma unroll
    for (int t = 0; t < 4; ++t) f[t] = tr_frag(lds, src + rd4[t], src + rd4[t] + 2048);
  };
  auto cnt_add = [&](unsigned a) { if (lane == 0) __hip_atomic_fetch_add((LAS unsigned*)(lds + a), 1u, __ATOMIC_RELAXED, __HIP_MEMORY_SCOPE_WORKGROUP); MEMFENCE(); };
  auto cnt_wait = [&](unsigned a) {
#pragma nounroll
    for (int it = 0; it < (1 << 20); ++it) {
      MEMFENCE();
      const unsigned v = *(volatile LAS unsigned*)(lds + a);
      if (__builtin_amdgcn_readfirstlane(v) >= 8u) break;
      __builtin_amdgcn_s_sleep(1);
    }
    MEMFENCE();
  };
  issue_w2(0, slot);
  unsigned tw3 = 0;
  {
    const unsigned k = (unsigned)(40 * J + 5 * w) + (unsigned)((lane < 40 ? lane : 39) >> 3);
    GLD1(tw3, (k < 512u ? 16u * (k >> 3) + 8u + (k & 7u) : k + 512u) * 1024u + (unsigned)(lane & 7) * 128u, w2, 0);
  }
  {
    const int rho0 = 4 * g + q;
    s16x8 f[6];
#pragma unroll
    for (int i = 0; i < 2; ++i) {
      const unsigned base = L_G32 + (unsigned)((2 * w + i) * 3072);
#pragma unroll
      for (int b = 0; b < 2; ++b) { const unsigned a = base + (unsigned)(64 * rho0 + 16 * (pp ^ ((rho0 >> 2) & 1)) + 8 * (b ^ (pp & 1))); f[i * 3 + b] = tr_frag(lds, a, a + 1024); }
      const unsigned a = base + 2048u + (unsigned)(32 * rho0 + 8 * pp);
      f[i * 3 + 2] = tr_frag(lds, a, a + 512);
    }
    WAIT_LGKM();
#pragma unroll
    for (int i = 0; i < 6; ++i) *(LAS s16x8*)(lds + L_G32 + w * 6144 + i * 1024 + l16) = f[i];
  }
  if (w == 6) lin64(13);
  bar();
  TS(35);
  if (w < 7) {
    const unsigned a0 = (w < 4 ? (unsigned)(w * 1024) : L_G32 + (unsigned)((w - 4) * 1024)) + l16, st = w < 4 ? 4096u : 3072u;
    f32x4 c0 = zero4, c1 = zero4;
#pragma unroll
    for (int i = 0; i < 16; i += 2) {
      c0 = mfma32(*(LAS s16x8*)(lds + a0 + i * st), ones, c0);
      c1 = mfma32(*(LAS s16x8*)(lds + a0 + (i + 1) * st), ones, c1);
    }
    if (r == 0) {
      *(LAS f32x4*)(lds + slot + 4096 + g * 32) = c0 + c1;
      *(LAS f32x4*)(lds + slot + 4096 + g * 32 + 16) = (f32x4){lo_f(bb1.x), hi_f(bb1.x), lo_f(bb1.y), hi_f(bb1.y)};
    }
  } else if (lane == 0) *(LAS u32x2*)(lds + L_CNT) = (u32x2){0u, 0u};
  WAIT_VM(T4B);
  read_w2(wr[0], slot);
  bar();
  issue_w2(1, slot);
  TS(36);
  const int tile3 = P * 8 + w;
  wait_flags(ws, WS_F3 + (unsigned)((tile3 * 16 + r) * 128), tlo, thi);
  TS(9);
  asm volatile("" : "+v"(tw3));
  {
    u32x4 hf[16];
    u32x2 sv[4];
    const char* hbp = ws + WS_H + (unsigned)(tile3 * 16384);
    {
      const char* sp = ws + WS_HS + (unsigned)(tile3 * 2048);
      const unsigned l8 = (unsigned)lane * 8u;
#pragma unroll
      for (int i = 0; i < 4; ++i) GLD2S(sv[i], l8, sp, i * 512);
    }
#pragma unroll
    for (int i = 0; i < 16; ++i) GLD4S(hf[i], l16, hbp + (i >> 2) * 4096, (i & 3) * 1024);
    read_w2(wr[1], slot);
    if (w < 4) __builtin_amdgcn_s_setprio(3); else __builtin_amdgcn_s_setprio(2);
    f32x4 acc[7];
#pragma unroll
    for (int t = 0; t < 7; ++t) acc[t] = zero4;
    {
      s16x8 f[2][4];
#pragma unroll
      for (int i = 0; i <= 16; ++i) {
        if (i < 16) {
#pragma unroll
          for (int t = 0; t < 4; ++t) f[i & 1][t] = *(LAS s16x8*)(lds + i * 4096 + t * 1024 + l16);
        }
        if (i > 0) {
          const int i0 = i - 1;
#pragma unroll
          for (int t = 0; t < 4; ++t) acc[t] = mfma32(f[i0 & 1][t], as_frag(hf[i0]), acc[t]);
          if (i < 16) ILVX(4, 1, 0);
        }
        SCHED_FENCE();
        if (i < 16) WAIT_VM1(15 - i, hf[i]);
      }
    }
    TS(37);
    cnt_add(L_CNT);
    issue_w2(2, slot);
    asm volatile("" : "+v"(sv[0]), "+v"(sv[1]), "+v"(sv[2]), "+v"(sv[3]));
    float rr, mrr;
    {
      const float sp = (__uint_as_float(sv[0].x) + __uint_as_float(sv[1].x)) + (__uint_as_float(sv[2].x) + __uint_as_float(sv[3].x));
      const float qp = (__uint_as_float(sv[0].y) + __uint_as_float(sv[1].y)) + (__uint_as_float(sv[2].y) + __uint_as_float(sv[3].y));
      const float S = mfma4(1.f, sp, zero4)[0], Q = mfma4(1.f, qp, zero4)[0];
      const float mean = S * (1.f / 512.f);
      const float var = fmaxf(Q * (1.f / 512.f) - mean * mean, 0.f);
      const float r1 = __builtin_amdgcn_rsqf(var + 1e-5f);
      rr = r1 * __builtin_amdgcn_rsqf(var * r1 * r1 + 1e-5f);
      mrr = -mean * rr;
    }
    if (w < 4) __builtin_amdgcn_s_setprio(1);
    TS(38);
    auto epi = [&](int t) -> u32x2 {
      const f32x4 cs = *(LAS f32x4*)(lds + L_SP + t * 6144 + 4096 + g * 32), bv = *(LAS f32x4*)(lds + L_SP + t * 6144 + 4096 + g * 32 + 16);
      const f32x4 v = acc[t] * rr + (cs * mrr + bv);
      const f32x2 a = gelu2((f32x2){v[0], v[1]}), b = gelu2((f32x2){v[2], v[3]});
      return (u32x2){pk_bf16(a.x, a.y), pk_bf16(b.x, b.y)};
    };
    const unsigned dst = WS_HH + (unsigned)((tile3 * 8 + (C >> 1)) * 7168);
    const unsigned dstc = dst + (unsigned)((C & 1) * 3072) + l16;
    const unsigned foff = (unsigned)((tile3 * 16 + C) * 128);
    auto pass2 = [&](int i0) {
      s16x8 f[12];
#pragma unroll
      for (int i = 0; i < 12; ++i) f[i] = *(LAS s16x8*)(lds + L_G32 + i0 * 3072 + i * 1024 + l16);
#pragma unroll
      for (int i = 0; i < 4; ++i)
#pragma unroll
        for (int t = 0; t < 3; ++t) acc[4 + t] = mfma32(f[3 * i + t], as_frag(hf[i0 + i]), acc[4 + t]);
    };
    {
      pass2(0);
      const u32x2 e0 = epi(0);
      P2ILV();
      SCHED_FENCE();
      pass2(4);
      const u32x2 e1 = epi(1);
      P2ILV();
      st16_sc1(rs, dstc, (u32x4){e0.x, e0.y, e1.x, e1.y});
      SCHED_FENCE();
    }
    TS(39);
    {
      pass2(8);
      const u32x2 e2 = epi(2);
      P2ILV();
      SCHED_FENCE();
      pass2(12);
      const u32x2 e3 = epi(3);
      P2ILV();
      st16_sc1(rs, dstc + 1024, (u32x4){e2.x, e2.y, e3.x, e3.y});
      SCHED_FENCE();
    }
    cnt_add(L_CNT + 4);
    const u32x2 e4 = epi(4);
    SCHED_FENCE();
    WAIT_VM(0);
    if (lane == 0) st8_sc1(rs, WS_F4A + foff, (u32x2){tlo, thi});
    TS(10);
    read_w2(wr[2], slot);
    {
      const u32x2 e5 = epi(5);
      st16_sc1(rs, dstc + 2048, (u32x4){e4.x, e4.y, e5.x, e5.y});
      const u32x2 e6 = epi(6);
      st8_sc1(rs, dst + 6144 + l16 + (unsigned)((C & 1) * 8), e6);
    }
    WAIT_LGKM();
    issue_w2(3, slot);
    TS(40);
    WAIT_VM(4);
    if (lane == 0) st8_sc1(rs, WS_F4B + foff, (u32x2){tlo, thi});
    TS(11);
    __builtin_amdgcn_s_setprio(0);
    cnt_wait(L_CNT);
    issue_w2(4, (unsigned)(w * 8192));
    issue_w2(5, (unsigned)(w * 8192 + 4096));
    WAIT_VM(9);
    read_w2(wr[3], slot);
    WAIT_LGKM();
    issue_w2(6, slot);
  }
  TS(41);
  {
    const int tile4 = P * 8 + th * 4;
    const unsigned foff = (unsigned)(((tile4 + ((lane >> 1) & 3)) * 16 + 2 * w + (lane & 1)) * 128);
    const char* ab = ws + WS_HH + (unsigned)((tile4 * 8 + w) * 7168) + 4096;
    u32x4 af[7][4];
#define LD_A(n) do { _Pragma("unroll") for (int tt = 0; tt < 4; ++tt) GLD4S(af[n][tt], l16, ab + tt * 57344, ((n) < 6 ? ((n) & 1) * 3072 + ((n) >> 1) * 1024 : 6144) - 4096); } while (0)
#define MM_A(n, cnt) do { _Pragma("unroll") for (int h2 = 0; h2 < 2; ++h2) { WAIT_VM2((cnt) + 2 - 2 * h2, af[n][2 * h2], af[n][2 * h2 + 1]); \
      _Pragma("unroll") for (int tt = 2 * h2; tt < 2 * h2 + 2; ++tt) _Pragma("unroll") for (int t = 0; t < 4; ++t) acc[tt][t] = mfma32(wr[(n) & 3][t], as_frag(af[n][tt]), (n) == 0 ? zero4 : acc[tt][t]); \
      SCHED_FENCE(); } } while (0)
    wait_flags(ws, WS_F4A + foff, tlo, thi);
    TS(12);
    u32x2 fb;
    f32x4 acc[4][4];
    LD_A(0); LD_A(1);
    GLD2S(fb, foff, ws + WS_F4B, 0);
    LD_A(2); LD_A(3);
    MM_A(0, 13);
    MM_A(1, 9);
    WAIT_VM1(8, fb);
    if (__builtin_amdgcn_ballot_w64((fb.x == tlo) & (fb.y == thi)) != ~0ull) wait_flags(ws, WS_F4B + foff, tlo, thi);
    TS(42);
    LD_A(4); LD_A(5);
    read_w2(wr[0], (unsigned)(w * 8192));
    read_w2(wr[1], (unsigned)(w * 8192 + 4096));
    MM_A(2, 12);
    LD_A(6);
    read_w2(wr[2], slot);
    MM_A(3, 12);
    MM_A(4, 8);
    MM_A(5, 4);
    MM_A(6, 0);
    TS(44);
    cnt_wait(L_CNT + 4);
    f32x4 k0 = zero4, k1 = zero4;
#pragma unroll
    for (int d = 0; d < 8; ++d) {
      if (d == w) { k0 = acc[d >> 1][2 * (d & 1)]; k1 = acc[d >> 1][2 * (d & 1) + 1]; }
      else {
        const int i = (d - w - 1) & 7;
        LAS char* dst = lds + (i < 4 ? (unsigned)(w * 8192 + i * 2048) : i < 6 ? slot + (unsigned)((i - 4) * 2048) : L_G32 + (unsigned)(w * 6144)) + l16;
        *(LAS f32x4*)dst = acc[d >> 1][2 * (d & 1)];
        *(LAS f32x4*)(dst + 1024) = acc[d >> 1][2 * (d & 1) + 1];
      }
    }
    bar();
    TS(13);
#pragma unroll
    for (int i = 0; i < 7; ++i) {
      const int e = (w - 1 - i) & 7;
      LAS char* src = lds + (i < 4 ? (unsigned)(e * 8192 + i * 2048) : i < 6 ? L_SP + (unsigned)(e * 6144 + (i - 4) * 2048) : L_G32 + (unsigned)(e * 6144)) + l16;
      k0 += *(LAS f32x4*)src;
      k1 += *(LAS f32x4*)(src + 1024);
    }
    const f32x4 A = (g & 1) ? k1 : k0, B = (g & 1) ? k0 : k1;
    u32x4 o;
    o.x = pk_bf16(lo_f(hb.x) + A[0] + lo_f(bb2.x), hi_f(hb.x) + A[1] + hi_f(bb2.x));
    o.y = pk_bf16(lo_f(hb.y) + A[2] + lo_f(bb2.y), hi_f(hb.y) + A[3] + hi_f(bb2.y));
    o.z = pk_bf16(lo_f(hb.z) + B[0] + lo_f(bb2.z), hi_f(hb.z) + B[1] + hi_f(bb2.z));
    o.w = pk_bf16(lo_f(hb.w) + B[2] + lo_f(bb2.w), hi_f(hb.w) + B[3] + hi_f(bb2.w));
    const unsigned xo_off = (unsigned)(((tilef * 16 + r) * 512 + ct * 64 + 16 * g + 8 * khf) * 2);
    *(u32x4*)((char*)out + xo_off) = o;
  }
  TS(14);
  asm volatile(".Lfb_code1:");
}

extern "C" void kernel_launch(void* const* d_in, const int* in_sizes, int n_in, void* d_out, int out_size, void* d_ws, size_t ws_size, hipStream_t stream) {
  const bf16_t* x = (const bf16_t*)d_in[0]; const bf16_t* wqkv = (const bf16_t*)d_in[1]; const bf16_t* wo = (const bf16_t*)d_in[2];
  const bf16_t* w1 = (const bf16_t*)d_in[4]; const bf16_t* b1 = (const bf16_t*)d_in[5]; const bf16_t* w2 = (const bf16_t*)d_in[6]; const bf16_t* b2 = (const bf16_t*)d_in[7];
  (void)hipFuncSetAttribute((const void*)fused_block, hipFuncAttributeMaxDynamicSharedMemorySize, 163840);
  fused_block<<<256, 512, 163840, stream>>>(x, wqkv, wo, w1, b1, w2, b2, (char*)d_ws, (bf16_t*)d_out);
}
```
